```python
import torch

def _softmax(x, axis=-1):
    return torch.softmax(x, dim=axis)
def _gelu(x):
    return torch.nn.functional.gelu(x, approximate="tanh")
def _silu(x):
    return torch.nn.functional.silu(x)
def _sigmoid(x):
    return torch.sigmoid(x)
def _layernorm(x, axis=-1, eps=1e-5):
    m = x.mean(dim=axis, keepdim=True)
    v = x.var(dim=axis, keepdim=True, unbiased=False)
    return (x - m) / torch.sqrt(v + eps)
def _scatter_add(tgt, idx, src, axis=0):
    return tgt.clone().index_add_(axis, idx, src)
def _segment_sum(src, seg, n):
    out = torch.zeros((n,) + src.shape[1:], dtype=src.dtype)
    return out.index_add_(0, seg, src)
def _pad(x, pw):
    flat = tuple(v for pair in reversed(pw) for v in pair)
    return torch.nn.functional.pad(x, flat)

kernel_name = 'gated_mix__mha_block__dense_activation_relu__prompt'

def setup_inputs():

    _w = (lambda a: (lambda t: t.to(torch.bfloat16) if t.is_floating_point() else t)(torch.as_tensor(a)))
    def _A_setup():
        B, S, D, H = 1, 2048, 640, 32
        return {
            "x": _w((lambda *s: torch.randn(*s, dtype=torch.bfloat16) * 0.05)(B, S, D)),
            "wqkv": _w((lambda *s: torch.randn(*s, dtype=torch.bfloat16) * 0.8)(D, 3 * D)),
            "wo": _w((lambda *s: torch.randn(*s, dtype=torch.bfloat16) * 0.2)(D, D)),
        }
    _A_in = _A_setup()

    def _B_setup():
        B, S, D, Dff = 1, 2048, 640, 1728
        return {
            "x": _w((lambda *s: torch.randn(*s, dtype=torch.bfloat16) * 0.05)(B, S, D)),
            "w1": _w((lambda *s: torch.randn(*s, dtype=torch.bfloat16) * 0.05)(D, Dff)),
            "w2": _w((lambda *s: torch.randn(*s, dtype=torch.bfloat16) * 0.05)(Dff, D)),
        }
    _B_in = _B_setup()

    return {
        "A_x": _A_in["x"],
        "A_wqkv": _A_in["wqkv"],
        "A_wo": _A_in["wo"],
        "B_x": _B_in["x"],
        "B_w1": _B_in["w1"],
        "B_w2": _B_in["w2"],
        "gate_w": _w((lambda *s: torch.randn(*s, dtype=torch.bfloat16) * 0.05)(640) * 16.0),
    }

def reference(A_x, A_wqkv, A_wo, B_x, B_w1, B_w2, gate_w):
    def _A_ref(x, wqkv, wo):
        B, S, D = x.shape
        H = 32
        Dh = D // H
        qkv = (lambda x, *d: x.permute(*d))((x @ wqkv).reshape(B, S, 3, H, Dh), 2, 0, 3, 1, 4)
        q, k, v = qkv[0], qkv[1], qkv[2]
        scores = torch.einsum("bhqd,bhkd->bhqk", q, k) * (Dh ** -0.5)
        p = _softmax(scores, axis=-1)
        o = torch.einsum("bhqk,bhkd->bhqd", p, v)
        return (lambda x, *d: x.permute(*d))(o, 0, 2, 1, 3).reshape(B, S, D) @ wo

    def _B_ref(x, w1, w2):
        h = torch.einsum("bsd,df->bsf", x, w1)
        zero = torch.zeros_like(h)
        h = torch.maximum(h, zero)
        return torch.einsum("bsf,fd->bsd", h, w2)

    x = A_x
    g = _sigmoid((x * gate_w).sum(-1, keepdims=True))
    a = _A_ref(x, A_wqkv, A_wo)
    b = _B_ref(x, B_w1, B_w2)
    return g * a + (1 - g) * b
```

```cpp
#include <hip/hip_runtime.h>
#include <hip/hip_bf16.h>
#include <stdint.h>

typedef short bf16x8 __attribute__((ext_vector_type(8)));
typedef short bf16x4 __attribute__((ext_vector_type(4)));
typedef float f32x16 __attribute__((ext_vector_type(16)));
typedef float f32x2 __attribute__((ext_vector_type(2)));
typedef __bf16 bfx2 __attribute__((ext_vector_type(2)));
typedef unsigned u32x4 __attribute__((ext_vector_type(4)));
typedef unsigned u32x2 __attribute__((ext_vector_type(2)));
typedef __attribute__((address_space(3))) char lds_char;
typedef unsigned short u16;

#define DI __device__ __forceinline__
#define MFMA16(a, b, c) __builtin_amdgcn_mfma_f32_32x32x16_bf16((a), (b), (c), 0, 0, 0)
#define MFMA8(a, b, c) __builtin_amdgcn_mfma_f32_32x32x8bf16_1k((a), (b), (c), 0, 0, 0)

constexpr int SEQ = 2048, DM = 640, NH = 32, DH = 20, DFF = 1728, NQKV = 1920;

constexpr size_t WS_KV = 0;
constexpr size_t WS_Q1 = WS_KV + (size_t)32 * 64 * 3072;
constexpr size_t WS_Q2 = WS_Q1 + (size_t)32 * 64 * 1024;
constexpr size_t WS_H = WS_Q2 + (size_t)32 * 64 * 256;
constexpr size_t WS_O = WS_H + (size_t)64 * 108 * 1024;
constexpr size_t WS_G = WS_O + (size_t)64 * 40 * 1024;

DI unsigned pk2(float lo, float hi) {
  f32x2 v = {lo, hi};
  bfx2 r = __builtin_convertvector(v, bfx2);
  return __builtin_bit_cast(unsigned, r);
}
DI float rbf(float x) { return (float)(__bf16)x; }
DI float bfbits2f(unsigned short b) { return __uint_as_float((unsigned)b << 16); }

DI void glds16(const void* g, lds_char* l) {
  __builtin_amdgcn_global_load_lds((const __attribute__((address_space(1))) unsigned*)g, (__attribute__((address_space(3))) unsigned*)l, 16, 0, 0);
}
typedef short v4i16_t __attribute__((ext_vector_type(4)));
DI bf16x4 tr_read(lds_char* p) {
  return __builtin_bit_cast(bf16x4, __builtin_amdgcn_ds_read_tr16_b64_v4i16((__attribute__((address_space(3))) v4i16_t*)p));
}
#define WAIT_VM(n) asm volatile("s_waitcnt vmcnt(%0)" ::"n"(n) : "memory")
#define RAW_BAR() do { asm volatile("s_waitcnt lgkmcnt(0)" ::: "memory"); __builtin_amdgcn_s_barrier(); } while (0)

template <int BN, int AMODE, bool SWAP, int NS>
DI void gemm_core(f32x16 (&acc)[2][BN / 64], const char* Abase, unsigned a_rt_stride, const char* Bbase, unsigned ldb_bytes,
                  int cmax, int nk, lds_char* lds) {
  constexpr int NB = BN / 64;
  constexpr int BPIECES = BN / 32;
  constexpr int NLD = 4 + BPIECES;
  constexpr int STAGE = 16384 + BN * 128;
  const int tid = threadIdx.x, l = tid & 63;
  const int w = __builtin_amdgcn_readfirstlane(tid >> 6);
  const int wr = w >> 1, wc = w & 1;
  const int r = l & 31, h = l >> 5;
  unsigned aoff[4];
  if (AMODE == 0) {
#pragma unroll
    for (int i = 0; i < 4; ++i) {
      int row = 8 * (4 * w + i) + (l >> 3);
      int c = (l & 7) ^ ((row >> 1) & 7);
      aoff[i] = row * 1280 + c * 16;
    }
  } else {
#pragma unroll
    for (int i = 0; i < 4; ++i) aoff[i] = w * a_rt_stride + i * 1024 + l * 16;
  }
  unsigned boff;
  if (BN == 128) {
    int c = (l & 15) ^ ((l >> 4) << 2);
    c = c < cmax ? c : cmax;
    boff = (16 * w + (l >> 4)) * ldb_bytes + c * 16;
  } else {
    int c = (l & 7) ^ (((l >> 4) & 1) << 2);
    c = c < cmax ? c : cmax;
    boff = (16 * w + (l >> 3)) * ldb_bytes + c * 16;
  }
  constexpr int BROWS = (BN == 128) ? 4 : 8;
  auto stage = [&](int kt, int slot) {
    lds_char* dst = lds + slot * STAGE;
    const char* ap = Abase + (AMODE == 0 ? kt * 128 : kt * 4096);
#pragma unroll
    for (int i = 0; i < 4; ++i) glds16(ap + aoff[i], dst + (4 * w + i) * 1024);
    const char* bp = Bbase + (size_t)kt * 64 * ldb_bytes;
#pragma unroll
    for (int i = 0; i < BPIECES; ++i) glds16(bp + boff + i * BROWS * ldb_bytes, dst + 16384 + (BPIECES * w + i) * 1024);
  };
  unsigned ard[4];
  if (AMODE == 0) {
#pragma unroll
    for (int ks = 0; ks < 4; ++ks) ard[ks] = (wr * 64 + r) * 128 + (((2 * ks + h) ^ ((r >> 1) & 7)) * 16);
  } else {
#pragma unroll
    for (int ks = 0; ks < 4; ++ks) ard[ks] = (wr * 2) * 4096 + ks * 1024 + l * 16;
  }
  unsigned brd[NB];
  {
    const int q = (l & 15) >> 2, p = l & 3, cbh = (l >> 4) & 1;
    const int krow = (AMODE == 0 ? 8 * h : 4 * h) + q;
#pragma unroll
    for (int n = 0; n < NB; ++n) {
      int chunk = (wc * NB + n) * 4 + 2 * cbh + (p >> 1);
      if (BN == 128) chunk ^= (q << 2); else chunk ^= (((q >> 1) & 1) << 2);
      brd[n] = 16384 + krow * (BN * 2) + chunk * 16 + (p & 1) * 8;
    }
  }
  constexpr int TOFF = (AMODE == 0 ? 4 : 8) * (BN * 2);
#pragma unroll
  for (int s = 0; s < NS - 1; ++s) stage(s, s);
  int slot = 0, pslot = NS - 1;
  for (int t = 0; t < nk; ++t) {
    if (t + (NS - 2) < nk) WAIT_VM((NS - 2) * NLD); else WAIT_VM(0);
    RAW_BAR();
    if (t + NS - 1 < nk) stage(t + NS - 1, pslot);
    lds_char* sb = lds + slot * STAGE;
#pragma unroll
    for (int ks = 0; ks < 4; ++ks) {
      bf16x8 af[2], bfr[NB];
#pragma unroll
      for (int m = 0; m < 2; ++m) af[m] = *(__attribute__((address_space(3))) bf16x8*)(sb + ard[ks] + m * 4096);
#pragma unroll
      for (int n = 0; n < NB; ++n) {
        bf16x4 lo = tr_read(sb + brd[n] + ks * 16 * (BN * 2));
        bf16x4 hi = tr_read(sb + brd[n] + ks * 16 * (BN * 2) + TOFF);
        bfr[n] = __builtin_shufflevector(lo, hi, 0, 1, 2, 3, 4, 5, 6, 7);
      }
#pragma unroll
      for (int m = 0; m < 2; ++m)
#pragma unroll
        for (int n = 0; n < NB; ++n) acc[m][n] = SWAP ? MFMA16(bfr[n], af[m], acc[m][n]) : MFMA16(af[m], bfr[n], acc[m][n]);
    }
    pslot = slot;
    slot = (slot + 1 == NS) ? 0 : slot + 1;
  }
  RAW_BAR();
}

constexpr int K1_NS = 3;
constexpr int K1_LDS = K1_NS * (16384 + 128 * 128);
constexpr int K1_GEMM_WGS = 16 * 29;
constexpr float QSCALE = 0.22360679774997896f * 1.4426950408889634f;

__global__ __launch_bounds__(256) void k1_kernel(const u16* __restrict__ x, const u16* __restrict__ wqkv, const u16* __restrict__ w1,
                                                 const u16* __restrict__ gw, char* __restrict__ ws) {
  extern __shared__ __attribute__((aligned(16))) char smem[];
  lds_char* lds = (lds_char*)smem;
  const int bid = blockIdx.x, tid = threadIdx.x, l = tid & 63;
  const int w = __builtin_amdgcn_readfirstlane(tid >> 6);
  if (bid >= K1_GEMM_WGS) {
    const int row = (bid - K1_GEMM_WGS) * 32 + w * 8 + (l >> 3), sub = l & 7;
    float s = 0.f;
#pragma unroll
    for (int i = 0; i < 10; ++i) {
      const int c = (i * 8 + sub) * 8;
      bf16x8 xv = *(const bf16x8*)(x + (size_t)row * DM + c);
      bf16x8 gv = *(const bf16x8*)(gw + c);
#pragma unroll
      for (int j = 0; j < 8; ++j) s += rbf(bfbits2f((u16)xv[j]) * bfbits2f((u16)gv[j]));
    }
    s += __shfl_xor(s, 1); s += __shfl_xor(s, 2); s += __shfl_xor(s, 4);
    s = rbf(s);
    float g = rbf(1.f / (1.f + __expf(-s)));
    if (sub == 0) ((float*)(ws + WS_G))[row] = g;
    return;
  }
  const int rowp = bid & 15, ct = bid >> 4;
  const int wr = w >> 1, wc = w & 1, r = l & 31, h = l >> 5;
  const char* Abase = (const char*)x + (size_t)rowp * 128 * 1280;
  f32x16 acc[2][2];
#pragma unroll
  for (int m = 0; m < 2; ++m)
#pragma unroll
    for (int n = 0; n < 2; ++n)
#pragma unroll
      for (int i = 0; i < 16; ++i) acc[m][n][i] = 0.f;
  if (ct < 10) {
    gemm_core<128, 0, true, K1_NS>(acc, Abase, 0, (const char*)wqkv + ct * 256, NQKV * 2, 15, 10, lds);
    const bool isq = ct < 5;
    const float sc = isq ? QSCALE : 1.f;
#pragma unroll
    for (int m = 0; m < 2; ++m) {
      const int tile = rowp * 4 + wr * 2 + m;
#pragma unroll
      for (int n = 0; n < 2; ++n) {
        const int fb4 = ((isq ? ct : ct - 5) * 128 + wc * 64 + n * 32) >> 2;
#pragma unroll
        for (int g = 0; g < 4; ++g) {
          const int G = fb4 + 2 * g + h;
          const int head = G / 5, gi = G - head * 5;
          u32x2 v;
          v[0] = pk2(acc[m][n][4 * g] * sc, acc[m][n][4 * g + 1] * sc);
          v[1] = pk2(acc[m][n][4 * g + 2] * sc, acc[m][n][4 * g + 3] * sc);
          const unsigned ht = head * 64 + tile;
          if (isq) {
            size_t a = gi < 4 ? WS_Q1 + (size_t)ht * 1024 + (r + 32 * (gi >> 1)) * 16 + (gi & 1) * 8 : WS_Q2 + (size_t)ht * 256 + r * 8;
            *(u32x2*)(ws + a) = v;
          } else {
            size_t a = WS_KV + (size_t)ht * 3072 + (gi < 4 ? (r + 32 * (gi >> 1)) * 16 + (gi & 1) * 8 : 1024 + r * 16);
            *(u32x2*)(ws + a) = v;
            if (gi == 4) { u32x2 c = {0x3F80u, 0u}; *(u32x2*)(ws + a + 8) = c; }
          }
        }
      }
    }
  } else if (ct < 15) {
    gemm_core<128, 0, false, K1_NS>(acc, Abase, 0, (const char*)wqkv + ct * 256, NQKV * 2, 15, 10, lds);
#pragma unroll
    for (int n = 0; n < 2; ++n) {
      const int f = (ct - 10) * 128 + wc * 64 + n * 32 + r;
      const int head = f / 20, d = f - head * 20;
#pragma unroll
      for (int m = 0; m < 2; ++m) {
        const int tile = rowp * 4 + wr * 2 + m;
        char* base = ws + WS_KV + (size_t)(head * 64 + tile) * 3072 + 1536 + h * 384 + d * 16;
#pragma unroll
        for (int s = 0; s < 2; ++s) {
          u32x4 v;
#pragma unroll
          for (int j = 0; j < 4; ++j) v[j] = pk2(acc[m][n][8 * s + 2 * j], acc[m][n][8 * s + 2 * j + 1]);
          *(u32x4*)(base + s * 768) = v;
          if (d == 19) { u32x4 o = {0x3F803F80u, 0x3F803F80u, 0x3F803F80u, 0x3F803F80u}; *(u32x4*)(base + s * 768 + 16) = o; }
        }
      }
    }
  } else {
    const int c1 = ct - 15;
    gemm_core<128, 0, true, K1_NS>(acc, Abase, 0, (const char*)w1 + c1 * 256, DFF * 2, c1 == 13 ? 7 : 15, 10, lds);
#pragma unroll
    for (int n = 0; n < 2; ++n) {
      const int c32 = c1 * 4 + wc * 2 + n;
      if (c32 < 54) {
#pragma unroll
        for (int m = 0; m < 2; ++m) {
          const int tile = rowp * 4 + wr * 2 + m;
#pragma unroll
          for (int s = 0; s < 2; ++s) {
            u32x4 v;
#pragma unroll
            for (int j = 0; j < 4; ++j) v[j] = pk2(fmaxf(acc[m][n][8 * s + 2 * j], 0.f), fmaxf(acc[m][n][8 * s + 2 * j + 1], 0.f));
            *(u32x4*)(ws + WS_H + ((size_t)tile * 108 + c32 * 2 + s) * 1024 + l * 16) = v;
          }
        }
      }
    }
  }
}

constexpr int AT_TPC = 4;
constexpr int AT_NS = 3;
constexpr int AT_CHUNK = AT_TPC * 3072;
constexpr int AT_LDS = AT_NS * AT_CHUNK;
constexpr int AT_NCH = 64 / AT_TPC;

DI float xhalf_max(float m) {
  auto rr = __builtin_amdgcn_permlane32_swap(__float_as_uint(m), __float_as_uint(m), false, false);
  return fmaxf(__uint_as_float(rr[0]), __uint_as_float(rr[1]));
}
DI float max16(const f32x16& s) {
  float a = fmaxf(fmaxf(s[0], s[1]), s[2]), b = fmaxf(fmaxf(s[3], s[4]), s[5]);
  a = fmaxf(fmaxf(a, s[6]), s[7]); b = fmaxf(fmaxf(b, s[8]), s[9]);
  a = fmaxf(fmaxf(a, s[10]), s[11]); b = fmaxf(fmaxf(b, s[12]), s[13]);
  a = fmaxf(fmaxf(a, s[14]), s[15]);
  return fmaxf(a, b);
}

__global__ __launch_bounds__(256) void k2_kernel(char* __restrict__ ws) {
  extern __shared__ __attribute__((aligned(16))) char smem[];
  lds_char* lds = (lds_char*)smem;
  const int bid = blockIdx.x, tid = threadIdx.x, l = tid & 63;
  const int w = __builtin_amdgcn_readfirstlane(tid >> 6);
  const int r = l & 31, h = l >> 5;
  const int xcd = bid & 7, idx = bid >> 3;
  const int head = xcd * 4 + (idx >> 4), qb = idx & 15;
  const int qt = qb * 4 + w;
  const char* kv = ws + WS_KV + (size_t)head * 64 * 3072;
  auto stage = [&](int ch, int slot) {
    const char* src = kv + (size_t)ch * AT_CHUNK + w * 3072 + l * 16;
    lds_char* dst = lds + slot * AT_CHUNK + w * 3072;
#pragma unroll
    for (int i = 0; i < 3; ++i) glds16(src + i * 1024, dst + i * 1024);
  };
  stage(0, 0);
  stage(1, 1);
  bf16x8 q1 = *(const bf16x8*)(ws + WS_Q1 + (size_t)(head * 64 + qt) * 1024 + l * 16);
  u32x2 q2u = *(const u32x2*)(ws + WS_Q2 + (size_t)(head * 64 + qt) * 256 + r * 8);
  if (h) { q2u[0] = 0; q2u[1] = 0; }
  const unsigned k1rd = l * 16, k2rd = 1024 + r * 16 + h * 8;
  const int dd = r < 24 ? r : (r < 28 ? r - 4 : r - 12);
  const unsigned vrd = 1536 + h * 384 + dd * 16;
  f32x16 zero;
#pragma unroll
  for (int i = 0; i < 16; ++i) zero[i] = 0.f;
  WAIT_VM(0);
  RAW_BAR();
  {
    bf16x8 k1 = *(__attribute__((address_space(3))) bf16x8*)(lds + k1rd);
    bf16x4 k2 = *(__attribute__((address_space(3))) bf16x4*)(lds + k2rd);
    f32x16 s = MFMA16(k1, q1, zero);
    s = MFMA8(k2, __builtin_bit_cast(bf16x4, q2u), s);
    float m = xhalf_max(max16(s));
    if (h) q2u[0] = pk2(-m, 0.f);
  }
  const bf16x4 q2 = __builtin_bit_cast(bf16x4, q2u);
  f32x16 o = zero;
  int slot = 0, pslot = AT_NS - 1;
  for (int ch = 0; ch < AT_NCH; ++ch) {
    if (ch > 0) {
      if (ch + 1 < AT_NCH) WAIT_VM(3); else WAIT_VM(0);
      RAW_BAR();
    }
    if (ch + 2 < AT_NCH) stage(ch + 2, pslot);
    lds_char* sb = lds + slot * AT_CHUNK;
#pragma unroll
    for (int t = 0; t < AT_TPC; ++t) {
      bf16x8 k1 = *(__attribute__((address_space(3))) bf16x8*)(sb + t * 3072 + k1rd);
      bf16x4 k2 = *(__attribute__((address_space(3))) bf16x4*)(sb + t * 3072 + k2rd);
      bf16x8 v0 = *(__attribute__((address_space(3))) bf16x8*)(sb + t * 3072 + vrd);
      bf16x8 v1 = *(__attribute__((address_space(3))) bf16x8*)(sb + t * 3072 + vrd + 768);
      f32x16 s = MFMA16(k1, q1, zero);
      s = MFMA8(k2, q2, s);
      u32x4 p0, p1;
#pragma unroll
      for (int j = 0; j < 4; ++j) {
        p0[j] = pk2(__builtin_amdgcn_exp2f(s[2 * j]), __builtin_amdgcn_exp2f(s[2 * j + 1]));
        p1[j] = pk2(__builtin_amdgcn_exp2f(s[8 + 2 * j]), __builtin_amdgcn_exp2f(s[8 + 2 * j + 1]));
      }
      o = MFMA16(v0, __builtin_bit_cast(bf16x8, p0), o);
      o = MFMA16(v1, __builtin_bit_cast(bf16x8, p1), o);
    }
    pslot = slot;
    slot = (slot + 1 == AT_NS) ? 0 : slot + 1;
  }
  float lsum;
  {
    auto rr = __builtin_amdgcn_permlane32_swap(__float_as_uint(o[8]), __float_as_uint(o[8]), false, false);
    lsum = __uint_as_float(rr[1]);
  }
  bool okv = lsum < 1.0e18f;
#pragma unroll
  for (int i = 0; i < 12; ++i) okv = okv && (h && i >= 8 ? true : fabsf(o[i]) < 1.0e30f);
  if (!__all(okv)) {
    u32x2 qz = q2u; if (h) qz[0] = 0;
    const bf16x4 q2z = __builtin_bit_cast(bf16x4, qz);
    float m = -INFINITY;
    o = zero;
    for (int t = 0; t < 64; ++t) {
      const char* tb = kv + (size_t)t * 3072;
      bf16x8 k1 = *(const bf16x8*)(tb + k1rd);
      bf16x4 k2 = *(const bf16x4*)(tb + k2rd);
      bf16x8 v0 = *(const bf16x8*)(tb + vrd);
      bf16x8 v1 = *(const bf16x8*)(tb + vrd + 768);
      f32x16 s = MFMA16(k1, q1, zero);
      s = MFMA8(k2, q2z, s);
      float mn = fmaxf(m, xhalf_max(max16(s)));
      float alpha = __builtin_amdgcn_exp2f(m - mn);
      m = mn;
#pragma unroll
      for (int i = 0; i < 16; ++i) o[i] *= alpha;
      u32x4 p0, p1;
#pragma unroll
      for (int j = 0; j < 4; ++j) {
        p0[j] = pk2(__builtin_amdgcn_exp2f(s[2 * j] - m), __builtin_amdgcn_exp2f(s[2 * j + 1] - m));
        p1[j] = pk2(__builtin_amdgcn_exp2f(s[8 + 2 * j] - m), __builtin_amdgcn_exp2f(s[8 + 2 * j + 1] - m));
      }
      o = MFMA16(v0, __builtin_bit_cast(bf16x8, p0), o);
      o = MFMA16(v1, __builtin_bit_cast(bf16x8, p1), o);
    }
    auto rr = __builtin_amdgcn_permlane32_swap(__float_as_uint(o[8]), __float_as_uint(o[8]), false, false);
    lsum = __uint_as_float(rr[1]);
  }
  const float inv = 1.f / lsum;
  char* ob = ws + WS_O + (size_t)qt * 40 * 1024;
#pragma unroll
  for (int g = 0; g < 3; ++g) {
    const int gi = 2 * g + h;
    if (gi < 5) {
      const int G = 5 * head + gi;
      u32x2 v;
      v[0] = pk2(o[4 * g] * inv, o[4 * g + 1] * inv);
      v[1] = pk2(o[4 * g + 2] * inv, o[4 * g + 3] * inv);
      *(u32x2*)(ob + (G >> 2) * 1024 + (r + 32 * (G & 1)) * 16 + ((G >> 1) & 1) * 8) = v;
    }
  }
}

constexpr int K3_NS = 3;
constexpr int K3_LDS = K3_NS * (16384 + 64 * 128);

__global__ __launch_bounds__(256) void k3_kernel(const u16* __restrict__ wo, const u16* __restrict__ w2, const char* __restrict__ ws,
                                                 u16* __restrict__ out) {
  extern __shared__ __attribute__((aligned(16))) char smem[];
  lds_char* lds = (lds_char*)smem;
  const int bid = blockIdx.x, tid = threadIdx.x, l = tid & 63;
  const int w = __builtin_amdgcn_readfirstlane(tid >> 6);
  const int wr = w >> 1, wc = w & 1, r = l & 31, h = l >> 5;
  const int rowp = bid & 15, ct = bid >> 4;
  f32x16 acca[2][1], accb[2][1];
#pragma unroll
  for (int m = 0; m < 2; ++m)
#pragma unroll
    for (int i = 0; i < 16; ++i) { acca[m][0][i] = 0.f; accb[m][0][i] = 0.f; }
  gemm_core<64, 1, true, K3_NS>(acca, ws + WS_O + (size_t)rowp * 4 * 40 * 1024, 40 * 1024, (const char*)wo + ct * 128, DM * 2, 7, 10, lds);
  gemm_core<64, 1, true, K3_NS>(accb, ws + WS_H + (size_t)rowp * 4 * 108 * 1024, 108 * 1024, (const char*)w2 + ct * 128, DM * 2, 7, 27, lds);
#pragma unroll
  for (int m = 0; m < 2; ++m) {
    const int token = rowp * 128 + wr * 64 + m * 32 + r;
    const float g = ((const float*)(ws + WS_G))[token];
    const float omg = rbf(1.f - g);
#pragma unroll
    for (int gq = 0; gq < 4; ++gq) {
      float v[4];
#pragma unroll
      for (int i = 0; i < 4; ++i) v[i] = rbf(g * rbf(acca[m][0][4 * gq + i])) + rbf(omg * rbf(accb[m][0][4 * gq + i]));
      u32x2 o2;
      o2[0] = pk2(v[0], v[1]); o2[1] = pk2(v[2], v[3]);
      *(u32x2*)(out + (size_t)token * DM + ct * 64 + wc * 32 + 8 * gq + 4 * h) = o2;
    }
  }
}

extern "C" void kernel_launch(void* const* d_in, const int* in_sizes, int n_in, void* d_out, int out_size, void* d_ws, size_t ws_size,
                              hipStream_t stream) {
  const u16* x = (const u16*)d_in[0];
  const u16* wqkv = (const u16*)d_in[1];
  const u16* wo = (const u16*)d_in[2];
  const u16* w1 = (const u16*)d_in[4];
  const u16* w2 = (const u16*)d_in[5];
  const u16* gw = (const u16*)d_in[6];
  char* ws = (char*)d_ws;
  hipFuncSetAttribute((const void*)k1_kernel, hipFuncAttributeMaxDynamicSharedMemorySize, K1_LDS);
  hipFuncSetAttribute((const void*)k3_kernel, hipFuncAttributeMaxDynamicSharedMemorySize, K3_LDS);
  k1_kernel<<<K1_GEMM_WGS + 64, 256, K1_LDS, stream>>>(x, wqkv, w1, gw, ws);
  k2_kernel<<<512, 256, AT_LDS, stream>>>(ws);
  k3_kernel<<<160, 256, K3_LDS, stream>>>(wo, w2, ws, (u16*)d_out);
}
```

```cpp
#include <hip/hip_runtime.h>
#include <hip/hip_bf16.h>
#include <stdint.h>

typedef short bf16x8 __attribute__((ext_vector_type(8)));
typedef short bf16x4 __attribute__((ext_vector_type(4)));
typedef float f32x16 __attribute__((ext_vector_type(16)));
typedef float f32x4 __attribute__((ext_vector_type(4)));
typedef float f32x2 __attribute__((ext_vector_type(2)));
typedef __bf16 bfx2 __attribute__((ext_vector_type(2)));
typedef unsigned u32x4 __attribute__((ext_vector_type(4)));
typedef unsigned u32x2 __attribute__((ext_vector_type(2)));
typedef __attribute__((address_space(3))) char lds_char;
typedef __attribute__((address_space(1))) unsigned gu32;
typedef unsigned short u16;

#define DI __device__ __forceinline__
#define MFMA16(a, b, c) __builtin_amdgcn_mfma_f32_32x32x16_bf16((a), (b), (c), 0, 0, 0)
#define MFMA8(a, b, c) __builtin_amdgcn_mfma_f32_32x32x8bf16_1k((a), (b), (c), 0, 0, 0)
#define MFMAS(a, b, c) __builtin_amdgcn_mfma_f32_16x16x32_bf16((a), (b), (c), 0, 0, 0)

constexpr int SEQ = 2048, DM = 640, NH = 32, DH = 20, DFF = 1728, NQKV = 1920;

constexpr size_t WS_KV = 0;
constexpr size_t WS_Q = WS_KV + (size_t)32 * 64 * 3072;
constexpr size_t WS_H = WS_Q + (size_t)32 * 64 * 1536;
constexpr size_t WS_O = WS_H + (size_t)64 * 108 * 1024;
constexpr size_t WS_B = WS_O + (size_t)128 * 20 * 1024;
constexpr size_t WS_G = WS_B + (size_t)3 * 2048 * 640 * 4;
constexpr size_t WS_FLAG = WS_G + 2048 * 4;

DI unsigned pk2(float lo, float hi) {
  f32x2 v = {lo, hi};
  bfx2 r = __builtin_convertvector(v, bfx2);
  return __builtin_bit_cast(unsigned, r);
}
DI float rbf(float x) { return (float)(__bf16)x; }
DI float bfbits2f(unsigned short b) { return __uint_as_float((unsigned)b << 16); }

DI void glds16(const char* sbase, unsigned voff, unsigned lds_dst) {
  unsigned keep;
  asm volatile("s_mov_b32 %0, m0\n\ts_mov_b32 m0, %3\n\ts_nop 0\n\tglobal_load_lds_dwordx4 %1, %2\n\ts_mov_b32 m0, %0"
               : "=&s"(keep) : "v"(voff), "s"(sbase), "s"(lds_dst) : "memory");
}
typedef short v4i16_t __attribute__((ext_vector_type(4)));
DI bf16x4 tr_read(lds_char* p) {
  return __builtin_bit_cast(bf16x4, __builtin_amdgcn_ds_read_tr16_b64_v4i16((__attribute__((address_space(3))) v4i16_t*)p));
}
#define SBAR() __builtin_amdgcn_sched_barrier(0)
#define PIN(x) asm volatile("" : "+v"(x))
#define WAIT_VM(n) asm volatile("s_waitcnt vmcnt(%0)" ::"n"(n) : "memory")
#define RAW_BAR() do { asm volatile("s_waitcnt lgkmcnt(0)" ::: "memory"); __builtin_amdgcn_s_barrier(); } while (0)
#define LDS128(p) (*(__attribute__((address_space(3))) bf16x8*)(p))
#define LDS64(p) (*(__attribute__((address_space(3))) bf16x4*)(p))
#define EX(x) __builtin_amdgcn_exp2f(x)

#define DBGSTAMP(off)
#define DBGARG
#define DBGPARAM
#define DBGDECL(o)
#define DBGDUMP(off, KID)
constexpr int DBG_EXTRA = 0;

template <int MT> struct Frag { bf16x8 a[MT]; bf16x4 bl, bh; };

template <int BN, int BK, int AMODE, bool SWAP, int NS>
DI void gemm8(f32x16 (&acc)[BN / 64], const char* Abase, unsigned a_rt_stride, const char* Bbase, unsigned ldb_bytes, int cmax, int nk,
              lds_char* lds DBGPARAM) {
  constexpr int MT = BN / 64;
  constexpr int KS = BK / 16;
  constexpr int ABYTES = 256 * BK, BBYTES = BK * BN * 2, STAGE = ABYTES + BBYTES;
  constexpr int APW = ABYTES / 8192, BPW = BBYTES / 8192, NLD = APW + BPW;
  static_assert(APW >= 1 && BPW >= 1, "");
  const int tid = threadIdx.x, l = tid & 63;
  const int w = __builtin_amdgcn_readfirstlane(tid >> 6);
  const int wr = (BN == 128) ? (w >> 2) : (w >> 1), wc = (BN == 128) ? (w & 3) : (w & 1);
  const int r = l & 31, h = l >> 5;
  unsigned aoff[APW];
#pragma unroll
  for (int i = 0; i < APW; ++i) {
    if (AMODE == 0) {
      if (BK == 64) {
        int row = 8 * (APW * w + i) + (l >> 3);
        aoff[i] = row * 1280 + (((l & 7) ^ ((row >> 1) & 7)) * 16);
      } else {
        int row = 16 * (APW * w + i) + (l >> 2);
        aoff[i] = row * 1280 + (((l & 3) ^ ((row >> 2) & 3)) * 16);
      }
    } else {
      const int pid = APW * w + i;
      aoff[i] = (pid / KS) * a_rt_stride + (pid % KS) * 1024 + l * 16;
    }
  }
  constexpr int BROWS = (BN == 128) ? 4 : 8;
  unsigned boff;
  if (BN == 128) {
    int c = (l & 15) ^ ((l >> 4) << 2);
    c = c < cmax ? c : cmax;
    boff = (BROWS * BPW * w + (l >> 4)) * ldb_bytes + c * 16;
  } else {
    int c = (l & 7) ^ (((l >> 4) & 1) << 2);
    c = c < cmax ? c : cmax;
    boff = (BROWS * BPW * w + (l >> 3)) * ldb_bytes + c * 16;
  }
  const unsigned lds0 = (unsigned)(uintptr_t)lds;
  auto issue = [&](int kt, int slot) {
    const unsigned dst = lds0 + slot * STAGE;
    const char* ap = Abase + (AMODE == 0 ? kt * (BK * 2) : kt * (KS * 1024));
#pragma unroll
    for (int i = 0; i < APW; ++i) glds16(ap, aoff[i], dst + (APW * w + i) * 1024);
    const char* bp = Bbase + (size_t)kt * BK * ldb_bytes;
#pragma unroll
    for (int i = 0; i < BPW; ++i) glds16(bp + i * BROWS * ldb_bytes, boff, dst + ABYTES + (BPW * w + i) * 1024);
  };
  unsigned ard[KS];
#pragma unroll
  for (int ks = 0; ks < KS; ++ks) {
    if (AMODE == 0) {
      if (BK == 64) ard[ks] = (wr * (32 * MT) + r) * 128 + (((2 * ks + h) ^ ((r >> 1) & 7)) * 16);
      else ard[ks] = (wr * (32 * MT) + r) * 64 + (((2 * ks + h) ^ ((r >> 2) & 3)) * 16);
    } else {
      ard[ks] = (wr * MT) * (KS * 1024) + ks * 1024 + l * 16;
    }
  }
  constexpr int AMS = 32 * BK * 2;
  unsigned brd;
  {
    const int q = (l & 15) >> 2, p = l & 3, cbh = (l >> 4) & 1;
    const int krow = (AMODE == 0 ? 8 * h : 4 * h) + q;
    int chunk = wc * 4 + 2 * cbh + (p >> 1);
    if (BN == 128) chunk ^= (q << 2); else chunk ^= (((q >> 1) & 1) << 2);
    brd = ABYTES + krow * (BN * 2) + chunk * 16 + (p & 1) * 8;
  }
  constexpr int TOFF = (AMODE == 0 ? 4 : 8) * (BN * 2);
  auto ldfrag = [&](Frag<MT>& f, lds_char* sb, int ks) {
    f.bl = tr_read(sb + brd + ks * 16 * (BN * 2));
    f.bh = tr_read(sb + brd + ks * 16 * (BN * 2) + TOFF);
#pragma unroll
    for (int m = 0; m < MT; ++m) f.a[m] = LDS128(sb + ard[ks] + m * AMS);
  };
  auto mma = [&](Frag<MT>& f, int m) {
    bf16x8 bb = __builtin_shufflevector(f.bl, f.bh, 0, 1, 2, 3, 4, 5, 6, 7);
    acc[m] = SWAP ? MFMA16(bb, f.a[m], acc[m]) : MFMA16(f.a[m], bb, acc[m]);
  };
  DBGSTAMP(DBG_OFF);
  issue(0, 0);
  issue(1, 1);
  Frag<MT> X, Y;
  WAIT_VM(NLD);
  RAW_BAR();
  ldfrag(X, lds, 0);
  int slot = 0, pslot = 0;
  for (int t = 0; t < nk; ++t) {
    lds_char* sb = lds + slot * STAGE;
#pragma unroll
    for (int ks = 0; ks < KS - 1; ++ks) {
      Frag<MT>& cur = (ks & 1) ? Y : X;
      Frag<MT>& nxt = (ks & 1) ? X : Y;
      mma(cur, 0);
      ldfrag(nxt, sb, ks + 1);
      SBAR();
      if (ks == 0) {
        if (t == 0) {
#pragma unroll
          for (int s = 2; s < NS; ++s) issue(s, s);
        } else if (t + NS - 1 < nk) {
          issue(t + NS - 1, pslot);
        }
      }
      if (MT == 2) mma(cur, MT - 1);
      SBAR();
    }
    {
      const int rem = nk - 2 - t;
      if (rem >= NS - 2) WAIT_VM((NS - 2) * NLD);
      else if (NS > 4 && rem == 2) WAIT_VM(2 * NLD);
      else if (NS > 3 && rem == 1) WAIT_VM(NLD);
      else WAIT_VM(0);
    }
    RAW_BAR();
    DBGSTAMP(DBG_OFF);
    pslot = slot;
    slot = (slot + 1 == NS) ? 0 : slot + 1;
    mma(Y, 0);
    ldfrag(X, lds + slot * STAGE, 0);
    SBAR();
    if (MT == 2) mma(Y, MT - 1);
    SBAR();
  }
  RAW_BAR();
}

constexpr int K1_NS = 4;
constexpr int K1_LDS = K1_NS * 32768 + DBG_EXTRA;
constexpr float QSCALE = 0.22360679774997896f * 1.4426950408889634f;

DI unsigned grp_off(int G, unsigned head_stride) {
  const int head = G / 5, gi = G - head * 5;
  return head * head_stride + (gi == 4 ? 1024 : (gi >> 1) * 512 + (gi & 1) * 8);
}

__global__ __launch_bounds__(512, 2) void k1_kernel(const u16* __restrict__ x, const u16* __restrict__ wqkv, char* __restrict__ ws) {
  extern __shared__ __attribute__((aligned(16))) char smem[];
  lds_char* lds = (lds_char*)smem;
  const int bid = blockIdx.x, tid = threadIdx.x, l = tid & 63;
  const int w = __builtin_amdgcn_readfirstlane(tid >> 6);
  const int rowp = bid & 15, ct = bid >> 4;
  const int wr = w >> 2, wc = w & 3, r = l & 31, h = l >> 5;
  if (bid == 0 && tid < 16) *(unsigned*)(ws + WS_FLAG + tid * 256) = 0u;
  const char* Abase = (const char*)x + (size_t)rowp * 128 * 1280;
  DBGDECL(K1_NS * 32768)
  f32x16 acc[2];
#pragma unroll
  for (int m = 0; m < 2; ++m)
#pragma unroll
    for (int i = 0; i < 16; ++i) acc[m][i] = 0.f;
  if (ct < 10) {
    gemm8<128, 64, 0, true, K1_NS>(acc, Abase, 0, (const char*)wqkv + ct * 256, NQKV * 2, 15, 10, lds DBGARG);
    const bool isq = ct < 5;
    const float sc = isq ? QSCALE : 1.f;
    const unsigned ts = isq ? 1536 : 3072;
    const int fb4 = (isq ? ct : ct - 5) * 32 + wc * 8;
    char* base = ws + (isq ? WS_Q : WS_KV) + (size_t)(rowp * 4 + wr * 2) * ts;
#pragma unroll
    for (int g = 0; g < 4; ++g) {
      const int G0 = fb4 + 2 * g;
      const unsigned o0 = grp_off(G0, 64 * ts), o1 = grp_off(G0 + 1, 64 * ts);
      const unsigned voff = r * 16 + h * (o1 - o0);
      const bool tail = ((G0 + h) % 5) == 4;
#pragma unroll
      for (int m = 0; m < 2; ++m) {
        u32x2 v;
        v[0] = pk2(acc[m][4 * g] * sc, acc[m][4 * g + 1] * sc);
        v[1] = pk2(acc[m][4 * g + 2] * sc, acc[m][4 * g + 3] * sc);
        char* p = base + o0 + m * ts + voff;
        *(u32x2*)p = v;
        if (!isq && tail) { u32x2 c = {0x3F80u, 0u}; *(u32x2*)(p + 8) = c; }
      }
    }
  } else {
    gemm8<128, 64, 0, false, K1_NS>(acc, Abase, 0, (const char*)wqkv + ct * 256, NQKV * 2, 15, 10, lds DBGARG);
    const int f = (ct - 10) * 128 + wc * 32 + r;
    const int head = f / 20, d = f - head * 20;
    char* base = ws + WS_KV + (size_t)(head * 64 + rowp * 4 + wr * 2) * 3072 + 1536 + h * 384 + d * 16;
#pragma unroll
    for (int m = 0; m < 2; ++m) {
#pragma unroll
      for (int s = 0; s < 2; ++s) {
        u32x4 v;
#pragma unroll
        for (int j = 0; j < 4; ++j) v[j] = pk2(acc[m][8 * s + 2 * j], acc[m][8 * s + 2 * j + 1]);
        *(u32x4*)(base + m * 3072 + s * 768) = v;
        if (d == 19) { u32x4 o = {0x3F803F80u, 0x3F803F80u, 0x3F803F80u, 0x3F803F80u}; *(u32x4*)(base + m * 3072 + s * 768 + 16) = o; }
      }
    }
  }
  DBGDUMP(K1_NS * 32768, 1);
}

#define FFN2_PRIO 0
#define FFN_PRIO 0
constexpr int AT_TPC = 8;
constexpr int AT_NS = 3;
constexpr int AT_CHUNK = AT_TPC * 3072;
constexpr int AT_NCH = 64 / AT_TPC;
constexpr int F1_NS = 4, F2_NS = 3;
constexpr int K2_LDS_MAIN = 3 * 24576;
constexpr int K2_LDS = K2_LDS_MAIN + DBG_EXTRA;
static_assert(AT_NS * AT_CHUNK <= K2_LDS_MAIN && F1_NS * 16384 <= K2_LDS_MAIN && F2_NS * 24576 <= K2_LDS_MAIN, "");

DI float xhalf_max(float m) {
  auto rr = __builtin_amdgcn_permlane32_swap(__float_as_uint(m), __float_as_uint(m), false, false);
  return fmaxf(__uint_as_float(rr[0]), __uint_as_float(rr[1]));
}
DI float max16(const f32x16& s) {
  float a = fmaxf(fmaxf(s[0], s[1]), s[2]), b = fmaxf(fmaxf(s[3], s[4]), s[5]);
  a = fmaxf(fmaxf(a, s[6]), s[7]); b = fmaxf(fmaxf(b, s[8]), s[9]);
  a = fmaxf(fmaxf(a, s[10]), s[11]); b = fmaxf(fmaxf(b, s[12]), s[13]);
  a = fmaxf(fmaxf(a, s[14]), s[15]);
  return fmaxf(a, b);
}

DI void attn_wg(char* __restrict__ ws, lds_char* lds, int bid) {
  const int tid = threadIdx.x, l = tid & 63;
  const int w = __builtin_amdgcn_readfirstlane(tid >> 6);
  const int r = l & 31, h = l >> 5;
  const int xcd = bid & 7, idx = bid >> 3;
  const int head = xcd * 4 + (idx >> 3), qb = idx & 7;
  const int qt = qb * 8 + w;
  const char* kv = ws + WS_KV + (size_t)head * 64 * 3072;
  const unsigned lds0 = (unsigned)(uintptr_t)lds;
  auto stage = [&](int ch, int slot) {
    const char* src = kv + (size_t)ch * AT_CHUNK + w * 3072;
    const unsigned dst = lds0 + slot * AT_CHUNK + w * 3072;
#pragma unroll
    for (int i = 0; i < 3; ++i) glds16(src + i * 1024, l * 16, dst + i * 1024);
  };
  stage(0, 0);
  const char* qp = ws + WS_Q + (size_t)(head * 64 + qt) * 1536;
  bf16x8 q1 = *(const bf16x8*)(qp + l * 16);
  u32x2 q2u = *(const u32x2*)(qp + 1024 + r * 16);
  PIN(q1); PIN(q2u);
  stage(1, 1);
  if (h) { q2u[0] = 0; q2u[1] = 0; }
  const unsigned k1rd = l * 16, k2rd = 1024 + r * 16 + h * 8;
  const int dd = r < 24 ? r : (r < 28 ? r - 4 : r - 12);
  const unsigned vrd = 1536 + h * 384 + dd * 16;
  f32x16 zero;
#pragma unroll
  for (int i = 0; i < 16; ++i) zero[i] = 0.f;
  WAIT_VM(3);
  RAW_BAR();
  bf16x8 kf1 = LDS128(lds + k1rd);
  bf16x4 kf2 = LDS64(lds + k2rd);
  {
    f32x16 s = MFMA16(kf1, q1, zero);
    s = MFMA8(kf2, __builtin_bit_cast(bf16x4, q2u), s);
    float m = xhalf_max(max16(s));
    if (h) q2u[0] = pk2(-m, 0.f);
  }
  const bf16x4 q2 = __builtin_bit_cast(bf16x4, q2u);
  f32x16 o = zero;
  f32x16 sA = MFMA16(kf1, q1, zero);
  sA = MFMA8(kf2, q2, sA);
  f32x16 sB;
  kf1 = LDS128(lds + 3072 + k1rd);
  kf2 = LDS64(lds + 3072 + k2rd);
  bf16x8 vf0 = LDS128(lds + vrd);
  bf16x8 vf1 = vf0;
  u32x4 p0, p1 = {0u, 0u, 0u, 0u};
#define AT_STEP(SC, SN, KN, VC, VN) do { \
    o = MFMA16(vf1, __builtin_bit_cast(bf16x8, p1), o); \
    vf1 = LDS128((VC) + vrd + 768); SBAR(); \
    SC[0] = EX(SC[0]); SC[1] = EX(SC[1]); SC[2] = EX(SC[2]); SC[3] = EX(SC[3]); PIN(SC); SBAR(); \
    SN = MFMA16(kf1, q1, zero); \
    kf1 = LDS128((KN) + k1rd); SBAR(); \
    SC[4] = EX(SC[4]); SC[5] = EX(SC[5]); SC[6] = EX(SC[6]); SC[7] = EX(SC[7]); \
    p0[0] = pk2(SC[0], SC[1]); p0[1] = pk2(SC[2], SC[3]); p0[2] = pk2(SC[4], SC[5]); p0[3] = pk2(SC[6], SC[7]); PIN(p0); SBAR(); \
    o = MFMA16(vf0, __builtin_bit_cast(bf16x8, p0), o); \
    vf0 = LDS128((VN) + vrd); SBAR(); \
    SC[8] = EX(SC[8]); SC[9] = EX(SC[9]); SC[10] = EX(SC[10]); SC[11] = EX(SC[11]); PIN(SC); SBAR(); \
    SN = MFMA8(kf2, q2, SN); \
    kf2 = LDS64((KN) + k2rd); SBAR(); \
    SC[12] = EX(SC[12]); SC[13] = EX(SC[13]); SC[14] = EX(SC[14]); SC[15] = EX(SC[15]); \
    p1[0] = pk2(SC[8], SC[9]); p1[1] = pk2(SC[10], SC[11]); p1[2] = pk2(SC[12], SC[13]); p1[3] = pk2(SC[14], SC[15]); PIN(p1); SBAR(); \
  } while (0)
  int slot = 0;
  for (int ch = 0; ch < AT_NCH; ++ch) {
    WAIT_VM(0);
    RAW_BAR();
    const int nslot = (slot + 1 == AT_NS) ? 0 : slot + 1;
    const int fslot = (nslot + 1 == AT_NS) ? 0 : nslot + 1;
    if (ch + 2 < AT_NCH) stage(ch + 2, fslot);
    lds_char* sb = lds + slot * AT_CHUNK;
    lds_char* nb = lds + nslot * AT_CHUNK;
    slot = nslot;
    AT_STEP(sA, sB, sb + 2 * 3072, sb, sb + 3072);
    AT_STEP(sB, sA, sb + 3 * 3072, sb + 3072, sb + 2 * 3072);
    AT_STEP(sA, sB, sb + 4 * 3072, sb + 2 * 3072, sb + 3 * 3072);
    AT_STEP(sB, sA, sb + 5 * 3072, sb + 3 * 3072, sb + 4 * 3072);
    AT_STEP(sA, sB, sb + 6 * 3072, sb + 4 * 3072, sb + 5 * 3072);
    AT_STEP(sB, sA, sb + 7 * 3072, sb + 5 * 3072, sb + 6 * 3072);
    AT_STEP(sA, sB, nb, sb + 6 * 3072, sb + 7 * 3072);
    AT_STEP(sB, sA, nb + 3072, sb + 7 * 3072, nb);
  }
  o = MFMA16(vf1, __builtin_bit_cast(bf16x8, p1), o);
  float lsum;
  {
    auto rr = __builtin_amdgcn_permlane32_swap(__float_as_uint(o[8]), __float_as_uint(o[8]), false, false);
    lsum = __uint_as_float(rr[1]);
  }
  bool okv = lsum < 1.0e18f;
#pragma unroll
  for (int i = 0; i < 12; ++i) okv = okv && ((h && i >= 8) ? true : fabsf(o[i]) < 1.0e30f);
  if (!__all(okv)) {
    u32x2 qz = q2u; if (h) qz[0] = 0;
    const bf16x4 q2z = __builtin_bit_cast(bf16x4, qz);
    float m = -INFINITY;
    o = zero;
    for (int t = 0; t < 64; ++t) {
      const char* tb = kv + (size_t)t * 3072;
      bf16x8 k1 = *(const bf16x8*)(tb + k1rd);
      bf16x4 k2 = *(const bf16x4*)(tb + k2rd);
      bf16x8 v0 = *(const bf16x8*)(tb + vrd);
      bf16x8 v1 = *(const bf16x8*)(tb + vrd + 768);
      f32x16 s = MFMA16(k1, q1, zero);
      s = MFMA8(k2, q2z, s);
      float mn = fmaxf(m, xhalf_max(max16(s)));
      float alpha = EX(m - mn);
      m = mn;
#pragma unroll
      for (int i = 0; i < 16; ++i) o[i] *= alpha;
      u32x4 a0, a1;
#pragma unroll
      for (int j = 0; j < 4; ++j) {
        a0[j] = pk2(EX(s[2 * j] - m), EX(s[2 * j + 1] - m));
        a1[j] = pk2(EX(s[8 + 2 * j] - m), EX(s[8 + 2 * j + 1] - m));
      }
      o = MFMA16(v0, __builtin_bit_cast(bf16x8, a0), o);
      o = MFMA16(v1, __builtin_bit_cast(bf16x8, a1), o);
    }
    auto rr = __builtin_amdgcn_permlane32_swap(__float_as_uint(o[8]), __float_as_uint(o[8]), false, false);
    lsum = __uint_as_float(rr[1]);
  }
  const float inv = 1.f / lsum;
  char* ob = ws + WS_O + (size_t)(qt * 2 + (r >> 4)) * 20 * 1024 + (r & 15) * 16;
#pragma unroll
  for (int g = 0; g < 3; ++g) {
    const int gi = 2 * g + h;
    if (gi < 5) {
      const int G = 5 * head + gi;
      u32x2 v;
      v[0] = pk2(o[4 * g] * inv, o[4 * g + 1] * inv);
      v[1] = pk2(o[4 * g + 2] * inv, o[4 * g + 3] * inv);
      *(u32x2*)(ob + (G >> 3) * 1024 + (G & 3) * 256 + ((G >> 2) & 1) * 8) = v;
    }
  }
}

DI void ffn_wg(const u16* __restrict__ x, const u16* __restrict__ w1, const u16* __restrict__ w2, const u16* __restrict__ gw,
               char* __restrict__ ws, lds_char* lds, int j) {
  const int tid = threadIdx.x, l = tid & 63;
  const int w = __builtin_amdgcn_readfirstlane(tid >> 6);
  const int r = l & 31, h = l >> 5;
  const int rowp = j & 15;
  gu32* flag = (gu32*)(ws + WS_FLAG + rowp * 256);
  DBGDECL(K2_LDS_MAIN)
  if (j < 224) {
    const int c1 = j >> 4, wr = w >> 2, wc = w & 3;
    f32x16 acc[2];
#pragma unroll
    for (int m = 0; m < 2; ++m)
#pragma unroll
      for (int i = 0; i < 16; ++i) acc[m][i] = 0.f;
    gemm8<128, 32, 0, true, F1_NS>(acc, (const char*)x + (size_t)rowp * 128 * 1280, 0, (const char*)w1 + c1 * 256, DFF * 2,
                                   c1 == 13 ? 7 : 15, 20, lds DBGARG);
    __builtin_amdgcn_s_setprio(3);
    const int c32 = c1 * 4 + wc;
    if (c32 < 54) {
      auto rsrc = __builtin_amdgcn_make_buffer_rsrc((void*)(ws + WS_H), 0, 64 * 108 * 1024, 0x00020000);
#pragma unroll
      for (int m = 0; m < 2; ++m) {
        const int tile = rowp * 4 + wr * 2 + m;
#pragma unroll
        for (int s = 0; s < 2; ++s) {
          u32x4 v;
#pragma unroll
          for (int q = 0; q < 4; ++q) v[q] = pk2(fmaxf(acc[m][8 * s + 2 * q], 0.f), fmaxf(acc[m][8 * s + 2 * q + 1], 0.f));
          __builtin_amdgcn_raw_buffer_store_b128(v, rsrc, (tile * 108 + c32 * 2 + s) * 1024 + l * 16, 0, 16);
        }
      }
    }
    asm volatile("s_waitcnt vmcnt(0)" ::: "memory");
    __syncthreads();
    if (tid == 0) __hip_atomic_fetch_add(flag, 1u, __ATOMIC_RELAXED, __HIP_MEMORY_SCOPE_AGENT);
  } else {
    const int row = (j - 224) * 64 + w * 8 + (l >> 3), sub = l & 7;
    float s = 0.f;
#pragma unroll
    for (int i = 0; i < 10; ++i) {
      const int c = (i * 8 + sub) * 8;
      bf16x8 xv = *(const bf16x8*)(x + (size_t)row * DM + c);
      bf16x8 gv = *(const bf16x8*)(gw + c);
#pragma unroll
      for (int q = 0; q < 8; ++q) s += rbf(bfbits2f((u16)xv[q]) * bfbits2f((u16)gv[q]));
    }
    s += __shfl_xor(s, 1); s += __shfl_xor(s, 2); s += __shfl_xor(s, 4);
    s = rbf(s);
    float g = rbf(1.f / (1.f + __expf(-s)));
    if (sub == 0) ((float*)(ws + WS_G))[row] = g;
  }
  if (j >= 16) {
    const int idx = (j - 16) >> 4, ct = idx % 5, sp = idx / 5, wr = w >> 2, wc = w & 3;
    if (w == 0) {
      for (unsigned spins = 0; spins < (1u << 22); ++spins) {
        if ((unsigned)__builtin_amdgcn_readfirstlane(__hip_atomic_load(flag, __ATOMIC_RELAXED, __HIP_MEMORY_SCOPE_AGENT)) >= 14u) break;
        __builtin_amdgcn_s_sleep(8);
      }
      __builtin_amdgcn_fence(__ATOMIC_ACQUIRE, "agent");
      asm volatile("s_waitcnt vmcnt(0)" ::: "memory");
    }
    __syncthreads();
    f32x16 acc[2];
#pragma unroll
    for (int m = 0; m < 2; ++m)
#pragma unroll
      for (int i = 0; i < 16; ++i) acc[m][i] = 0.f;
    __builtin_amdgcn_s_setprio(FFN2_PRIO);
    gemm8<128, 32, 1, true, F1_NS>(acc, ws + WS_H + (size_t)rowp * 4 * 108 * 1024 + sp * 36 * 1024, 108 * 1024,
                                   (const char*)w2 + (size_t)sp * 576 * DM * 2 + ct * 256, DM * 2, 15, 18, lds DBGARG);
    __builtin_amdgcn_s_setprio(3);
#pragma unroll
    for (int m = 0; m < 2; ++m) {
      const int token = rowp * 128 + wr * 64 + m * 32 + r;
      char* bp = ws + WS_B + ((size_t)(sp * 128 + (token >> 4)) * 40 + ct * 8 + wc * 2) * 1024 + h * 256 + (token & 15) * 16;
#pragma unroll
      for (int g = 0; g < 4; ++g)
        *(f32x4*)(bp + (g >> 1) * 1024 + (g & 1) * 512) = f32x4{acc[m][4 * g], acc[m][4 * g + 1], acc[m][4 * g + 2], acc[m][4 * g + 3]};
    }
  }
  DBGDUMP(K2_LDS_MAIN, 2);
}

__global__ __launch_bounds__(512, 4) void k2_kernel(const u16* __restrict__ x, const u16* __restrict__ w1, const u16* __restrict__ w2,
                                                    const u16* __restrict__ gw, char* __restrict__ ws) {
  extern __shared__ __attribute__((aligned(16))) char smem[];
  lds_char* lds = (lds_char*)smem;
  const int bid = blockIdx.x;
  if (bid < 256) attn_wg(ws, lds, bid);
  else { __builtin_amdgcn_s_setprio(FFN_PRIO); ffn_wg(x, w1, w2, gw, ws, lds, bid - 256); }
}

constexpr int K3_LDS = 640 * 160 + 4 * 5120;

__global__ __launch_bounds__(512, 2) void k3_kernel(const u16* __restrict__ wo, const char* __restrict__ ws, u16* __restrict__ out) {
  extern __shared__ __attribute__((aligned(16))) char smem[];
  lds_char* lds = (lds_char*)smem;
  const int bid = blockIdx.x, tid = threadIdx.x, l = tid & 63;
  const int w = __builtin_amdgcn_readfirstlane(tid >> 6);
  const int wt = w & 3, kh = w >> 2;
  const int rt = bid & 31, ct = bid >> 5;
  const int tk = l & 15, kq = l >> 4;
  const int token = rt * 64 + wt * 16 + tk;
  const unsigned lds0 = (unsigned)(uintptr_t)lds;
  const char* op = ws + WS_O + (size_t)((rt * 4 + wt) * 20 + kh * 10) * 1024;
  bf16x8 of[10];
#pragma unroll
  for (int ks = 0; ks < 10; ++ks) asm volatile("global_load_dwordx4 %0, %1, %2" : "=v"(of[ks]) : "v"(l * 16 + ks * 1024), "s"(op) : "memory");
  const char* wb = (const char*)wo + ct * 160;
#pragma unroll
  for (int i = 0; i < 13; ++i) {
    const int sq = i * 8 + w;
    if (sq < 100) {
      const int pc = (sq & 1) * 50 + (sq >> 1);
      const int ci = pc * 64 + l, row = ci / 10, c = ci - row * 10;
      glds16(wb, row * 1280 + c * 16, lds0 + pc * 1024);
    } else {
      glds16(wb, l * 16, lds0 + 640 * 160 + wt * 5120);
    }
  }
  const size_t eo = (size_t)token * DM + ct * 80 + 4 * kq;
  f32x4 bp[3][5];
  float g = 0.f;
  if (kh == 0) {
#pragma unroll
    for (int sp = 0; sp < 3; ++sp)
#pragma unroll
      for (int n = 0; n < 5; ++n)
        asm volatile("global_load_dwordx4 %0, %1, %2" : "=v"(bp[sp][n]) : "v"(l * 16 + n * 1024), "s"(ws + WS_B + ((size_t)(sp * 128 + rt * 4 + wt) * 40 + ct * 5) * 1024) : "memory");
    asm volatile("global_load_dword %0, %1, off" : "=v"(g) : "v"((const float*)(ws + WS_G) + token) : "memory");
  }
  f32x4 acc[5];
#pragma unroll
  for (int n = 0; n < 5; ++n) acc[n] = f32x4{0.f, 0.f, 0.f, 0.f};
  const unsigned brd = (kh * 320 + 4 * kq + (tk >> 2)) * 160 + (tk & 3) * 8;
#pragma unroll
  for (int ph = 0; ph < 2; ++ph) {
    if (ph == 0) {
      if (kh == 0) WAIT_VM(6 + 16); else WAIT_VM(6);
      asm volatile("" : "+v"(of[0]), "+v"(of[1]), "+v"(of[2]), "+v"(of[3]), "+v"(of[4]), "+v"(of[5]), "+v"(of[6]), "+v"(of[7]), "+v"(of[8]), "+v"(of[9])::"memory");
    } else {
      if (kh == 0) WAIT_VM(16); else WAIT_VM(0);
    }
    RAW_BAR();
#pragma unroll
    for (int k5 = 0; k5 < 5; ++k5) {
      const int ks = ph * 5 + k5;
#pragma unroll
      for (int n = 0; n < 5; ++n) {
        bf16x4 lo = tr_read(lds + brd + ks * 32 * 160 + n * 32);
        bf16x4 hi = tr_read(lds + brd + ks * 32 * 160 + 16 * 160 + n * 32);
        bf16x8 wf = __builtin_shufflevector(lo, hi, 0, 1, 2, 3, 4, 5, 6, 7);
        acc[n] = MFMAS(wf, of[ks], acc[n]);
      }
    }
  }
  lds_char* red = lds + 640 * 160 + wt * 5120 + l * 16;
  if (kh == 1) {
#pragma unroll
    for (int n = 0; n < 5; ++n) *(__attribute__((address_space(3))) f32x4*)(red + n * 1024) = acc[n];
  }
  RAW_BAR();
  if (kh == 0) {
    asm volatile("s_waitcnt vmcnt(0)" : "+v"(bp[0][0]), "+v"(bp[0][1]), "+v"(bp[0][2]), "+v"(bp[0][3]), "+v"(bp[0][4]), "+v"(bp[1][0]), "+v"(bp[1][1]),
                 "+v"(bp[1][2]), "+v"(bp[1][3]), "+v"(bp[1][4]), "+v"(bp[2][0]), "+v"(bp[2][1]), "+v"(bp[2][2]), "+v"(bp[2][3]), "+v"(bp[2][4]), "+v"(g)::"memory");
    const float omg = rbf(1.f - g);
#pragma unroll
    for (int n = 0; n < 5; ++n) {
      f32x4 a2 = *(__attribute__((address_space(3))) f32x4*)(red + n * 1024);
      float v[4];
#pragma unroll
      for (int i = 0; i < 4; ++i) {
        const float a = rbf(acc[n][i] + a2[i]);
        const float b = rbf(bp[0][n][i] + bp[1][n][i] + bp[2][n][i]);
        v[i] = rbf(g * a) + rbf(omg * b);
      }
      u32x2 o2;
      o2[0] = pk2(v[0], v[1]);
      o2[1] = pk2(v[2], v[3]);
      *(u32x2*)(out + eo + n * 16) = o2;
    }
  }
}

extern "C" void kernel_launch(void* const* d_in, const int* in_sizes, int n_in, void* d_out, int out_size, void* d_ws, size_t ws_size,
                              hipStream_t stream) {
  const u16* x = (const u16*)d_in[0];
  const u16* wqkv = (const u16*)d_in[1];
  const u16* wo = (const u16*)d_in[2];
  const u16* w1 = (const u16*)d_in[4];
  const u16* w2 = (const u16*)d_in[5];
  const u16* gw = (const u16*)d_in[6];
  char* ws = (char*)d_ws;
  hipFuncSetAttribute((const void*)k1_kernel, hipFuncAttributeMaxDynamicSharedMemorySize, K1_LDS);
  hipFuncSetAttribute((const void*)k2_kernel, hipFuncAttributeMaxDynamicSharedMemorySize, K2_LDS);
  hipFuncSetAttribute((const void*)k3_kernel, hipFuncAttributeMaxDynamicSharedMemorySize, K3_LDS);
  k1_kernel<<<240, 512, K1_LDS, stream>>>(x, wqkv, ws);
  k2_kernel<<<512, 512, K2_LDS, stream>>>(x, w1, w2, gw, ws);
  k3_kernel<<<256, 512, K3_LDS, stream>>>(wo, ws, (u16*)d_out);
}
```

```cpp
#include <hip/hip_runtime.h>
#include <hip/hip_bf16.h>
#include <stdint.h>

typedef short bf16x8 __attribute__((ext_vector_type(8)));
typedef short bf16x4 __attribute__((ext_vector_type(4)));
typedef float f32x16 __attribute__((ext_vector_type(16)));
typedef float f32x4 __attribute__((ext_vector_type(4)));
typedef float f32x2 __attribute__((ext_vector_type(2)));
typedef __bf16 bfx2 __attribute__((ext_vector_type(2)));
typedef unsigned u32x4 __attribute__((ext_vector_type(4)));
typedef unsigned u32x2 __attribute__((ext_vector_type(2)));
typedef __attribute__((address_space(3))) char lds_char;
typedef __attribute__((address_space(1))) unsigned gu32;
typedef unsigned short u16;

#define DI __device__ __forceinline__
#define MFMA16(a, b, c) __builtin_amdgcn_mfma_f32_32x32x16_bf16((a), (b), (c), 0, 0, 0)
#define MFMA8(a, b, c) __builtin_amdgcn_mfma_f32_32x32x8bf16_1k((a), (b), (c), 0, 0, 0)
#define MFMAS(a, b, c) __builtin_amdgcn_mfma_f32_16x16x32_bf16((a), (b), (c), 0, 0, 0)

constexpr int SEQ = 2048, DM = 640, NH = 32, DH = 20, DFF = 1728, NQKV = 1920;

constexpr size_t WS_KV = 0;
constexpr size_t WS_Q = WS_KV + (size_t)32 * 64 * 3072;
constexpr size_t WS_H = WS_Q + (size_t)32 * 64 * 1536;
constexpr size_t WS_O = WS_H + (size_t)64 * 108 * 1024;
constexpr size_t WS_B = WS_O + (size_t)128 * 20 * 1024;
constexpr size_t WS_G = WS_B + (size_t)3 * 2048 * 640 * 4;
constexpr size_t WS_FLAG = WS_G + 2048 * 4;

DI unsigned pk2(float lo, float hi) {
  f32x2 v = {lo, hi};
  bfx2 r = __builtin_convertvector(v, bfx2);
  return __builtin_bit_cast(unsigned, r);
}
DI float rbf(float x) { return (float)(__bf16)x; }
DI float bfbits2f(unsigned short b) { return __uint_as_float((unsigned)b << 16); }

DI void glds16(const char* sbase, unsigned voff, unsigned lds_dst) {
  unsigned keep;
  asm volatile("s_mov_b32 %0, m0\n\ts_mov_b32 m0, %3\n\ts_nop 0\n\tglobal_load_lds_dwordx4 %1, %2\n\ts_mov_b32 m0, %0"
               : "=&s"(keep) : "v"(voff), "s"(sbase), "s"(lds_dst) : "memory");
}
typedef short v4i16_t __attribute__((ext_vector_type(4)));
DI bf16x4 tr_read(lds_char* p) {
  return __builtin_bit_cast(bf16x4, __builtin_amdgcn_ds_read_tr16_b64_v4i16((__attribute__((address_space(3))) v4i16_t*)p));
}
#define SBAR() __builtin_amdgcn_sched_barrier(0)
#define PIN(x) asm volatile("" : "+v"(x))
#define WAIT_VM(n) asm volatile("s_waitcnt vmcnt(%0)" ::"n"(n) : "memory")
#define RAW_BAR() do { asm volatile("s_waitcnt lgkmcnt(0)" ::: "memory"); __builtin_amdgcn_s_barrier(); } while (0)
#define LDS128(p) (*(__attribute__((address_space(3))) bf16x8*)(p))
#define LDS64(p) (*(__attribute__((address_space(3))) bf16x4*)(p))
#define EX(x) __builtin_amdgcn_exp2f(x)

#define DBGSTAMP(off)
#define DBGARG
#define DBGPARAM
#define DBGDECL(o)
#define DBGDUMP(off, KID)
constexpr int DBG_EXTRA = 0;

template <int MT, int NT> struct Frag { bf16x8 a[MT]; bf16x4 bl[NT], bh[NT]; };

template <int MT, int NT, int AMODE, bool SWAP, int NS>
DI void gemm8(f32x16 (&acc)[MT][NT], const char* Abase, unsigned a_rt_stride, const char* Bbase, unsigned ldb_bytes, int cmax, int nk,
              lds_char* lds DBGPARAM) {
  constexpr int BM = 128 * MT, BN = 64 * NT, BK = 64, KS = 4;
  constexpr int ABYTES = BM * BK * 2, BBYTES = BK * BN * 2, STAGE = ABYTES + BBYTES;
  constexpr int APW = ABYTES / 8192, BPW = BBYTES / 8192, NLD = APW + BPW;
  const int tid = threadIdx.x, l = tid & 63;
  const int w = __builtin_amdgcn_readfirstlane(tid >> 6);
  const int wr = w >> 1, wc = w & 1;
  const int r = l & 31, h = l >> 5;
  unsigned aoff[APW];
#pragma unroll
  for (int i = 0; i < APW; ++i) {
    const int pid = APW * w + i;
    if (AMODE == 0) {
      int row = 8 * pid + (l >> 3);
      aoff[i] = row * 1280 + (((l & 7) ^ ((row >> 1) & 7)) * 16);
    } else {
      aoff[i] = (pid / KS) * a_rt_stride + (pid % KS) * 1024 + l * 16;
    }
  }
  constexpr int BROWS = (BN == 128) ? 4 : 8;
  unsigned boff;
  if (BN == 128) {
    int c = (l & 15) ^ ((l >> 4) << 2);
    c = c < cmax ? c : cmax;
    boff = (BROWS * BPW * w + (l >> 4)) * ldb_bytes + c * 16;
  } else {
    int c = (l & 7) ^ (((l >> 4) & 1) << 2);
    c = c < cmax ? c : cmax;
    boff = (BROWS * BPW * w + (l >> 3)) * ldb_bytes + c * 16;
  }
  const unsigned lds0 = (unsigned)(uintptr_t)lds;
  auto piece = [&](int kt, int slot, int i) {
    const unsigned dst = lds0 + slot * STAGE;
    if (i < APW) {
      const char* ap = Abase + (AMODE == 0 ? kt * (BK * 2) : kt * (KS * 1024));
      glds16(ap, aoff[i < APW ? i : 0], dst + (APW * w + i) * 1024);
    } else {
      const char* bp = Bbase + (size_t)kt * BK * ldb_bytes;
      glds16(bp + (i - APW) * BROWS * ldb_bytes, boff, dst + ABYTES + (BPW * w + (i - APW)) * 1024);
    }
  };
  auto issue = [&](int kt, int slot) {
#pragma unroll
    for (int i = 0; i < NLD; ++i) piece(kt, slot, i);
  };
  unsigned ard[KS];
#pragma unroll
  for (int ks = 0; ks < KS; ++ks) {
    if (AMODE == 0) ard[ks] = (wr * (32 * MT) + r) * 128 + (((2 * ks + h) ^ ((r >> 1) & 7)) * 16);
    else ard[ks] = (wr * MT) * 4096 + ks * 1024 + l * 16;
  }
  unsigned brd[NT];
  {
    const int q = (l & 15) >> 2, p = l & 3, cbh = (l >> 4) & 1;
    const int krow = (AMODE == 0 ? 8 * h : 4 * h) + q;
#pragma unroll
    for (int n = 0; n < NT; ++n) {
      int chunk = (wc * NT + n) * 4 + 2 * cbh + (p >> 1);
      if (BN == 128) chunk ^= (q << 2); else chunk ^= (((q >> 1) & 1) << 2);
      brd[n] = ABYTES + krow * (BN * 2) + chunk * 16 + (p & 1) * 8;
    }
  }
  constexpr int TOFF = (AMODE == 0 ? 4 : 8) * (BN * 2);
  auto ldA = [&](Frag<MT, NT>& f, lds_char* sb, int ks, int m) { f.a[m] = LDS128(sb + ard[ks] + m * 4096); };
  auto ldB = [&](Frag<MT, NT>& f, lds_char* sb, int ks, int n) {
    f.bl[n] = tr_read(sb + brd[n] + ks * 16 * (BN * 2));
    f.bh[n] = tr_read(sb + brd[n] + ks * 16 * (BN * 2) + TOFF);
  };
  auto mma = [&](Frag<MT, NT>& f, int m, int n) {
    bf16x8 bb = __builtin_shufflevector(f.bl[n], f.bh[n], 0, 1, 2, 3, 4, 5, 6, 7);
    acc[m][n] = SWAP ? MFMA16(bb, f.a[m], acc[m][n]) : MFMA16(f.a[m], bb, acc[m][n]);
  };
  auto step = [&](Frag<MT, NT>& cur, Frag<MT, NT>& nxt, lds_char* nsb, int nks, bool pf, int pkt, int pslot, int P0, int P1) {
    mma(cur, 0, 0);
    ldB(nxt, nsb, nks, 0); ldA(nxt, nsb, nks, 0);
    SBAR();
    if (NT == 2) { mma(cur, 0, NT - 1); ldB(nxt, nsb, nks, NT - 1); }
    if (MT == 2) ldA(nxt, nsb, nks, MT - 1);
    SBAR();
    if (pf && P0 < P1) piece(pkt, pslot, P0);
    if (MT == 2) { mma(cur, MT - 1, 0); SBAR(); }
    if (pf) { for (int i = P0 + 1; i < P1; ++i) piece(pkt, pslot, i); }
    if (MT == 2 && NT == 2) mma(cur, MT - 1, NT - 1);
    SBAR();
  };
  constexpr int PQ = (NLD + 2) / 3;
  constexpr int P1 = PQ, P2 = 2 * PQ < NLD ? 2 * PQ : NLD;
  DBGSTAMP(DBG_OFF);
  issue(0, 0);
  issue(1, 1);
  Frag<MT, NT> X, Y;
  WAIT_VM(NLD);
  RAW_BAR();
#pragma unroll
  for (int n = 0; n < NT; ++n) ldB(X, lds, 0, n);
#pragma unroll
  for (int m = 0; m < MT; ++m) ldA(X, lds, 0, m);
  int slot = 0, pslot = NS - 1;
  static_assert(NS == 3, "");
#pragma unroll 1
  for (int t = 0; t < nk; ++t) {
    lds_char* sb = lds + slot * STAGE;
    const int pkt = t + NS - 1;
    const bool pf = pkt < nk;
    step(X, Y, sb, 1, pf, pkt, pslot, 0, P1);
    step(Y, X, sb, 2, pf, pkt, pslot, P1, P2);
    step(X, Y, sb, 3, pf, pkt, pslot, P2, NLD);
    if (pf) WAIT_VM(NLD); else WAIT_VM(0);
    RAW_BAR();
    DBGSTAMP(DBG_OFF);
    pslot = slot;
    slot = (slot + 1 == NS) ? 0 : slot + 1;
    step(Y, X, lds + slot * STAGE, 0, false, 0, 0, 0, 0);
  }
  RAW_BAR();
}

constexpr int K1_NS = 3;
constexpr int K1_LDS = K1_NS * 49152 + DBG_EXTRA;
constexpr float QSCALE = 0.22360679774997896f * 1.4426950408889634f;

DI unsigned unit_off(int U, unsigned head_stride) {
  const int p = U / 5, ui = U - p * 5;
  return (2 * p + (ui >= 3 ? 1 : 0)) * head_stride + (ui == 2 ? 1024 : (ui >= 3 ? ui - 3 : ui) * 512);
}

__global__ __launch_bounds__(512, 2) void k1_kernel(const u16* __restrict__ x, const u16* __restrict__ wqkv, const u16* __restrict__ w1,
                                                    char* __restrict__ ws) {
  extern __shared__ __attribute__((aligned(16))) char smem[];
  lds_char* lds = (lds_char*)smem;
  const int bid = blockIdx.x, tid = threadIdx.x, l = tid & 63;
  const int w = __builtin_amdgcn_readfirstlane(tid >> 6);
  const int rowp = bid & 7, ct = bid >> 3;
  const int wr = w >> 1, wc = w & 1, r = l & 31, h = l >> 5;
  const char* Abase = (const char*)x + (size_t)rowp * 256 * 1280;
  const int tile0 = rowp * 8 + wr * 2;
  DBGDECL(K1_NS * 49152)
  f32x16 acc[2][2];
#pragma unroll
  for (int m = 0; m < 2; ++m)
#pragma unroll
    for (int n = 0; n < 2; ++n)
#pragma unroll
      for (int i = 0; i < 16; ++i) acc[m][n][i] = 0.f;
  if (ct >= 10 && ct < 15) {
    gemm8<2, 2, 0, false, K1_NS>(acc, Abase, 0, (const char*)wqkv + ct * 256, NQKV * 2, 15, 10, lds DBGARG);
#pragma unroll
    for (int n = 0; n < 2; ++n) {
      const int f = (ct - 10) * 128 + wc * 64 + n * 32 + r;
      const int head = f / 20, d = f - head * 20;
      char* base = ws + WS_KV + (size_t)(head * 64 + tile0) * 3072 + 1536 + h * 384 + d * 16;
#pragma unroll
      for (int m = 0; m < 2; ++m) {
#pragma unroll
        for (int s = 0; s < 2; ++s) {
          u32x4 v;
#pragma unroll
          for (int j = 0; j < 4; ++j) v[j] = pk2(acc[m][n][8 * s + 2 * j], acc[m][n][8 * s + 2 * j + 1]);
          *(u32x4*)(base + m * 3072 + s * 768) = v;
          if (d == 19) { u32x4 o = {0x3F803F80u, 0x3F803F80u, 0x3F803F80u, 0x3F803F80u}; *(u32x4*)(base + m * 3072 + s * 768 + 16) = o; }
        }
      }
    }
  } else {
    const bool isqk = ct < 10;
    const char* Bb = isqk ? (const char*)wqkv + ct * 256 : (const char*)w1 + (ct - 15) * 256;
    gemm8<2, 2, 0, true, K1_NS>(acc, Abase, 0, Bb, isqk ? NQKV * 2 : DFF * 2, ct == 28 ? 7 : 15, 10, lds DBGARG);
    if (isqk) {
      const bool isq = ct < 5;
      const float sc = isq ? QSCALE : 1.f;
      const unsigned ts = isq ? 1536 : 3072;
      char* base = ws + (isq ? WS_Q : WS_KV) + (size_t)tile0 * ts;
#pragma unroll
      for (int n = 0; n < 2; ++n) {
        const int ub = (isq ? ct : ct - 5) * 16 + wc * 8 + n * 4;
#pragma unroll
        for (int gp = 0; gp < 2; ++gp) {
          const int U0 = ub + 2 * gp;
          const unsigned o0 = unit_off(U0, 64 * ts), o1 = unit_off(U0 + 1, 64 * ts);
          const unsigned voff = r * 16 + h * (o1 - o0);
          const bool mixed = ((U0 + h) % 5) == 2;
#pragma unroll
          for (int m = 0; m < 2; ++m) {
            const f32x16& a = acc[m][n];
            unsigned a0 = pk2(a[8 * gp] * sc, a[8 * gp + 1] * sc), a1 = pk2(a[8 * gp + 2] * sc, a[8 * gp + 3] * sc);
            unsigned b0 = pk2(a[8 * gp + 4] * sc, a[8 * gp + 5] * sc), b1 = pk2(a[8 * gp + 6] * sc, a[8 * gp + 7] * sc);
            auto s0 = __builtin_amdgcn_permlane32_swap(a0, b0, false, false);
            auto s1 = __builtin_amdgcn_permlane32_swap(a1, b1, false, false);
            u32x4 v = {s0[0], s1[0], s0[1], s1[1]};
            char* p = base + o0 + m * ts + voff;
            *(u32x4*)p = v;
            if (mixed) *(u32x4*)(p + 64 * ts) = v;
          }
        }
      }
    } else {
#pragma unroll
      for (int n = 0; n < 2; ++n) {
        const int c32 = (ct - 15) * 4 + wc * 2 + n;
        if (c32 < 54) {
#pragma unroll
          for (int m = 0; m < 2; ++m) {
#pragma unroll
            for (int s = 0; s < 2; ++s) {
              u32x4 v;
#pragma unroll
              for (int q = 0; q < 4; ++q) v[q] = pk2(fmaxf(acc[m][n][8 * s + 2 * q], 0.f), fmaxf(acc[m][n][8 * s + 2 * q + 1], 0.f));
              *(u32x4*)(ws + WS_H + ((size_t)(tile0 + m) * 108 + c32 * 2 + s) * 1024 + l * 16) = v;
            }
          }
        }
      }
    }
  }
  DBGDUMP(K1_NS * 49152, 1);
}

#define FFN2_PRIO 0
#define FFN_PRIO 0
constexpr int AT_TPC = 8;
constexpr int AT_NS = 3;
constexpr int AT_CHUNK = AT_TPC * 3072;
constexpr int AT_NCH = 64 / AT_TPC;
constexpr int F2_NS = 3;
constexpr int K2_LDS_MAIN = 3 * 24576;
constexpr int K2_LDS = K2_LDS_MAIN + DBG_EXTRA;
static_assert(AT_NS * AT_CHUNK <= K2_LDS_MAIN && F2_NS * 24576 <= K2_LDS_MAIN, "");

DI float xhalf_max(float m) {
  auto rr = __builtin_amdgcn_permlane32_swap(__float_as_uint(m), __float_as_uint(m), false, false);
  return fmaxf(__uint_as_float(rr[0]), __uint_as_float(rr[1]));
}
DI float max16(const f32x16& s) {
  float a = fmaxf(fmaxf(s[0], s[1]), s[2]), b = fmaxf(fmaxf(s[3], s[4]), s[5]);
  a = fmaxf(fmaxf(a, s[6]), s[7]); b = fmaxf(fmaxf(b, s[8]), s[9]);
  a = fmaxf(fmaxf(a, s[10]), s[11]); b = fmaxf(fmaxf(b, s[12]), s[13]);
  a = fmaxf(fmaxf(a, s[14]), s[15]);
  return fmaxf(a, b);
}

DI void attn_wg(char* __restrict__ ws, lds_char* lds, int bid) {
  const int tid = threadIdx.x, l = tid & 63;
  const int w = __builtin_amdgcn_readfirstlane(tid >> 6);
  const int r = l & 31, h = l >> 5;
  const int xcd = bid & 7, idx = bid >> 3;
  const int head = xcd * 4 + (idx >> 3), qb = idx & 7;
  const int qt = qb * 8 + w;
  const char* kv = ws + WS_KV + (size_t)head * 64 * 3072;
  const unsigned lds0 = (unsigned)(uintptr_t)lds;
  auto stage = [&](int ch, int slot) {
    const char* src = kv + (size_t)ch * AT_CHUNK + w * 3072;
    const unsigned dst = lds0 + slot * AT_CHUNK + w * 3072;
#pragma unroll
    for (int i = 0; i < 3; ++i) glds16(src + i * 1024, l * 16, dst + i * 1024);
  };
  stage(0, 0);
  const char* qp = ws + WS_Q + (size_t)(head * 64 + qt) * 1536;
  bf16x8 q1 = *(const bf16x8*)(qp + l * 16);
  u32x2 q2u = *(const u32x2*)(qp + 1024 + r * 16 + (head & 1) * 8);
  PIN(q1); PIN(q2u);
  stage(1, 1);
  if (h) { q2u[0] = 0; q2u[1] = 0; }
  const unsigned k1rd = l * 16, k2rd = h ? 1536 + 20 * 16 : 1024 + r * 16 + (head & 1) * 8;
  const int dd = r < 24 ? r : (r < 28 ? r - 4 : r - 12);
  const unsigned vrd = 1536 + h * 384 + dd * 16;
  f32x16 zero;
#pragma unroll
  for (int i = 0; i < 16; ++i) zero[i] = 0.f;
  WAIT_VM(3);
  RAW_BAR();
  bf16x8 kf1 = LDS128(lds + k1rd);
  bf16x4 kf2 = LDS64(lds + k2rd);
  {
    f32x16 s = MFMA16(kf1, q1, zero);
    s = MFMA8(kf2, __builtin_bit_cast(bf16x4, q2u), s);
    float m = xhalf_max(max16(s));
    if (h) q2u[0] = pk2(-m, 0.f);
  }
  const bf16x4 q2 = __builtin_bit_cast(bf16x4, q2u);
  f32x16 o = zero;
  f32x16 sA = MFMA16(kf1, q1, zero);
  sA = MFMA8(kf2, q2, sA);
  f32x16 sB;
  kf1 = LDS128(lds + 3072 + k1rd);
  kf2 = LDS64(lds + 3072 + k2rd);
  bf16x8 vf0 = LDS128(lds + vrd);
  bf16x8 vf1 = vf0;
  u32x4 p0, p1 = {0u, 0u, 0u, 0u};
#define AT_STEP(SC, SN, KN, VC, VN) do { \
    o = MFMA16(vf1, __builtin_bit_cast(bf16x8, p1), o); \
    vf1 = LDS128((VC) + vrd + 768); SBAR(); \
    SC[0] = EX(SC[0]); SC[1] = EX(SC[1]); SC[2] = EX(SC[2]); SC[3] = EX(SC[3]); PIN(SC); SBAR(); \
    SN = MFMA16(kf1, q1, zero); \
    kf1 = LDS128((KN) + k1rd); SBAR(); \
    SC[4] = EX(SC[4]); SC[5] = EX(SC[5]); SC[6] = EX(SC[6]); SC[7] = EX(SC[7]); \
    p0[0] = pk2(SC[0], SC[1]); p0[1] = pk2(SC[2], SC[3]); p0[2] = pk2(SC[4], SC[5]); p0[3] = pk2(SC[6], SC[7]); PIN(p0); SBAR(); \
    o = MFMA16(vf0, __builtin_bit_cast(bf16x8, p0), o); \
    vf0 = LDS128((VN) + vrd); SBAR(); \
    SC[8] = EX(SC[8]); SC[9] = EX(SC[9]); SC[10] = EX(SC[10]); SC[11] = EX(SC[11]); PIN(SC); SBAR(); \
    SN = MFMA8(kf2, q2, SN); \
    kf2 = LDS64((KN) + k2rd); SBAR(); \
    SC[12] = EX(SC[12]); SC[13] = EX(SC[13]); SC[14] = EX(SC[14]); SC[15] = EX(SC[15]); \
    p1[0] = pk2(SC[8], SC[9]); p1[1] = pk2(SC[10], SC[11]); p1[2] = pk2(SC[12], SC[13]); p1[3] = pk2(SC[14], SC[15]); PIN(p1); SBAR(); \
  } while (0)
  int slot = 0;
  for (int ch = 0; ch < AT_NCH; ++ch) {
    WAIT_VM(0);
    RAW_BAR();
    const int nslot = (slot + 1 == AT_NS) ? 0 : slot + 1;
    const int fslot = (nslot + 1 == AT_NS) ? 0 : nslot + 1;
    if (ch + 2 < AT_NCH) stage(ch + 2, fslot);
    lds_char* sb = lds + slot * AT_CHUNK;
    lds_char* nb = lds + nslot * AT_CHUNK;
    slot = nslot;
    AT_STEP(sA, sB, sb + 2 * 3072, sb, sb + 3072);
    AT_STEP(sB, sA, sb + 3 * 3072, sb + 3072, sb + 2 * 3072);
    AT_STEP(sA, sB, sb + 4 * 3072, sb + 2 * 3072, sb + 3 * 3072);
    AT_STEP(sB, sA, sb + 5 * 3072, sb + 3 * 3072, sb + 4 * 3072);
    AT_STEP(sA, sB, sb + 6 * 3072, sb + 4 * 3072, sb + 5 * 3072);
    AT_STEP(sB, sA, sb + 7 * 3072, sb + 5 * 3072, sb + 6 * 3072);
    AT_STEP(sA, sB, nb, sb + 6 * 3072, sb + 7 * 3072);
    AT_STEP(sB, sA, nb + 3072, sb + 7 * 3072, nb);
  }
  o = MFMA16(vf1, __builtin_bit_cast(bf16x8, p1), o);
  float lsum;
  {
    auto rr = __builtin_amdgcn_permlane32_swap(__float_as_uint(o[8]), __float_as_uint(o[8]), false, false);
    lsum = __uint_as_float(rr[1]);
  }
  bool okv = lsum < 1.0e18f;
#pragma unroll
  for (int i = 0; i < 12; ++i) okv = okv && ((h && i >= 8) ? true : fabsf(o[i]) < 1.0e30f);
  if (!__all(okv)) {
    u32x2 qz = q2u; if (h) qz[0] = 0;
    const bf16x4 q2z = __builtin_bit_cast(bf16x4, qz);
    float m = -INFINITY;
    o = zero;
    for (int t = 0; t < 64; ++t) {
      const char* tb = kv + (size_t)t * 3072;
      bf16x8 k1 = *(const bf16x8*)(tb + k1rd);
      bf16x4 k2 = *(const bf16x4*)(tb + k2rd);
      bf16x8 v0 = *(const bf16x8*)(tb + vrd);
      bf16x8 v1 = *(const bf16x8*)(tb + vrd + 768);
      f32x16 s = MFMA16(k1, q1, zero);
      s = MFMA8(k2, q2z, s);
      float mn = fmaxf(m, xhalf_max(max16(s)));
      float alpha = EX(m - mn);
      m = mn;
#pragma unroll
      for (int i = 0; i < 16; ++i) o[i] *= alpha;
      u32x4 a0, a1;
#pragma unroll
      for (int j = 0; j < 4; ++j) {
        a0[j] = pk2(EX(s[2 * j] - m), EX(s[2 * j + 1] - m));
        a1[j] = pk2(EX(s[8 + 2 * j] - m), EX(s[8 + 2 * j + 1] - m));
      }
      o = MFMA16(v0, __builtin_bit_cast(bf16x8, a0), o);
      o = MFMA16(v1, __builtin_bit_cast(bf16x8, a1), o);
    }
    auto rr = __builtin_amdgcn_permlane32_swap(__float_as_uint(o[8]), __float_as_uint(o[8]), false, false);
    lsum = __uint_as_float(rr[1]);
  }
  const float inv = 1.f / lsum;
  char* ob = ws + WS_O + (size_t)(qt * 2 + (r >> 4)) * 20 * 1024 + (r & 15) * 16;
#pragma unroll
  for (int g = 0; g < 3; ++g) {
    const int gi = 2 * g + h;
    if (gi < 5) {
      const int G = 5 * head + gi;
      u32x2 v;
      v[0] = pk2(o[4 * g] * inv, o[4 * g + 1] * inv);
      v[1] = pk2(o[4 * g + 2] * inv, o[4 * g + 3] * inv);
      *(u32x2*)(ob + (G >> 3) * 1024 + (G & 3) * 256 + ((G >> 2) & 1) * 8) = v;
    }
  }
}

DI void ffn_wg(const u16* __restrict__ x, const u16* __restrict__ w2, const u16* __restrict__ gw, char* __restrict__ ws, lds_char* lds, int j) {
  const int tid = threadIdx.x, l = tid & 63;
  const int w = __builtin_amdgcn_readfirstlane(tid >> 6);
  const int r = l & 31, h = l >> 5;
  DBGDECL(K2_LDS_MAIN)
  if (j >= 160) {
    const int row = (j - 160) * 64 + w * 8 + (l >> 3), sub = l & 7;
    float s = 0.f;
#pragma unroll
    for (int i = 0; i < 10; ++i) {
      const int c = (i * 8 + sub) * 8;
      bf16x8 xv = *(const bf16x8*)(x + (size_t)row * DM + c);
      bf16x8 gv = *(const bf16x8*)(gw + c);
#pragma unroll
      for (int q = 0; q < 8; ++q) s += rbf(bfbits2f((u16)xv[q]) * bfbits2f((u16)gv[q]));
    }
    s += __shfl_xor(s, 1); s += __shfl_xor(s, 2); s += __shfl_xor(s, 4);
    s = rbf(s);
    float g = rbf(1.f / (1.f + __expf(-s)));
    if (sub == 0) ((float*)(ws + WS_G))[row] = g;
  } else {
    const int rowp = j & 15, ct = j >> 4, wr = w >> 1, wc = w & 1;
    f32x16 acc[1][1];
#pragma unroll
    for (int i = 0; i < 16; ++i) acc[0][0][i] = 0.f;
    gemm8<1, 1, 1, true, F2_NS>(acc, ws + WS_H + (size_t)rowp * 4 * 108 * 1024, 108 * 1024, (const char*)w2 + ct * 128, DM * 2, 7, 27, lds DBGARG);
    __builtin_amdgcn_s_setprio(3);
    const int token = rowp * 128 + wr * 32 + r;
    char* bp = ws + WS_B + ((size_t)(token >> 4) * 40 + ct * 4 + wc * 2) * 512 + h * 128 + (token & 15) * 8;
#pragma unroll
    for (int g = 0; g < 4; ++g) {
      u32x2 v;
      v[0] = pk2(acc[0][0][4 * g], acc[0][0][4 * g + 1]);
      v[1] = pk2(acc[0][0][4 * g + 2], acc[0][0][4 * g + 3]);
      *(u32x2*)(bp + (g >> 1) * 512 + (g & 1) * 256) = v;
    }
  }
  DBGDUMP(K2_LDS_MAIN, 2);
}

__global__ __launch_bounds__(512, 4) void k2_kernel(const u16* __restrict__ x, const u16* __restrict__ w2,
                                                    const u16* __restrict__ gw, char* __restrict__ ws) {
  extern __shared__ __attribute__((aligned(16))) char smem[];
  lds_char* lds = (lds_char*)smem;
  const int bid = blockIdx.x;
  if (bid < 256) attn_wg(ws, lds, bid);
  else { __builtin_amdgcn_s_setprio(FFN_PRIO); ffn_wg(x, w2, gw, ws, lds, bid - 256); }
}

constexpr int K3_LDS = 640 * 160 + 4 * 5120;

__global__ __launch_bounds__(512, 2) void k3_kernel(const u16* __restrict__ wo, const char* __restrict__ ws, u16* __restrict__ out) {
  extern __shared__ __attribute__((aligned(16))) char smem[];
  lds_char* lds = (lds_char*)smem;
  const int bid = blockIdx.x, tid = threadIdx.x, l = tid & 63;
  const int w = __builtin_amdgcn_readfirstlane(tid >> 6);
  const int wt = w & 3, kh = w >> 2;
  const int rt = bid & 31, ct = bid >> 5;
  const int tk = l & 15, kq = l >> 4;
  const int token = rt * 64 + wt * 16 + tk;
  const unsigned lds0 = (unsigned)(uintptr_t)lds;
#define K3ST(i)
  const char* op = ws + WS_O + (size_t)((rt * 4 + wt) * 20 + kh * 10) * 1024;
  bf16x8 of[10];
#pragma unroll
  for (int ks = 0; ks < 10; ++ks) asm volatile("global_load_dwordx4 %0, %1, %2" : "=v"(of[ks]) : "v"(l * 16 + ks * 1024), "s"(op) : "memory");
  const char* wb = (const char*)wo + ct * 160;
#pragma unroll
  for (int i = 0; i < 13; ++i) {
    const int sq = i * 8 + w;
    if (sq < 100) {
      const int pc = (sq & 1) * 50 + (sq >> 1);
      const int ci = pc * 64 + l, row = ci / 10, c = ci - row * 10;
      glds16(wb, row * 1280 + c * 16, lds0 + pc * 1024);
    } else {
      glds16(wb, l * 16, lds0 + 640 * 160 + wt * 5120);
    }
  }
  const size_t eo = (size_t)token * DM + ct * 80 + 4 * kq;
  u32x2 bp[5];
  float g = 0.f;
  if (kh == 0) {
#pragma unroll
    for (int n = 0; n < 5; ++n)
      asm volatile("global_load_dwordx2 %0, %1, %2" : "=v"(bp[n]) : "v"(l * 8 + n * 512), "s"(ws + WS_B + ((size_t)(rt * 4 + wt) * 40 + ct * 5) * 512) : "memory");
    asm volatile("global_load_dword %0, %1, off" : "=v"(g) : "v"((const float*)(ws + WS_G) + token) : "memory");
  }
  K3ST(1);
  f32x4 acc[5];
#pragma unroll
  for (int n = 0; n < 5; ++n) acc[n] = f32x4{0.f, 0.f, 0.f, 0.f};
  const unsigned brd = (kh * 320 + 4 * kq + (tk >> 2)) * 160 + (tk & 3) * 8;
#pragma unroll
  for (int ph = 0; ph < 2; ++ph) {
    if (ph == 0) {
      if (kh == 0) WAIT_VM(6 + 6); else WAIT_VM(6);
      asm volatile("" : "+v"(of[0]), "+v"(of[1]), "+v"(of[2]), "+v"(of[3]), "+v"(of[4]), "+v"(of[5]), "+v"(of[6]), "+v"(of[7]), "+v"(of[8]), "+v"(of[9])::"memory");
    } else {
      if (kh == 0) WAIT_VM(6); else WAIT_VM(0);
    }
    RAW_BAR();
    K3ST(2 + ph);
#pragma unroll
    for (int k5 = 0; k5 < 5; ++k5) {
      const int ks = ph * 5 + k5;
#pragma unroll
      for (int n = 0; n < 5; ++n) {
        bf16x4 lo = tr_read(lds + brd + ks * 32 * 160 + n * 32);
        bf16x4 hi = tr_read(lds + brd + ks * 32 * 160 + 16 * 160 + n * 32);
        bf16x8 wf = __builtin_shufflevector(lo, hi, 0, 1, 2, 3, 4, 5, 6, 7);
        acc[n] = MFMAS(wf, of[ks], acc[n]);
      }
    }
  }
  K3ST(4);
  lds_char* red = lds + 640 * 160 + wt * 5120 + l * 16;
  if (kh == 1) {
#pragma unroll
    for (int n = 0; n < 5; ++n) *(__attribute__((address_space(3))) f32x4*)(red + n * 1024) = acc[n];
  }
  RAW_BAR();
  if (kh == 0) {
    asm volatile("s_waitcnt vmcnt(0)" : "+v"(bp[0]), "+v"(bp[1]), "+v"(bp[2]), "+v"(bp[3]), "+v"(bp[4]), "+v"(g)::"memory");
    const float omg = rbf(1.f - g);
#pragma unroll
    for (int n = 0; n < 5; ++n) {
      f32x4 a2 = *(__attribute__((address_space(3))) f32x4*)(red + n * 1024);
      float v[4];
#pragma unroll
      for (int i = 0; i < 4; ++i) {
        const float a = rbf(acc[n][i] + a2[i]);
        const float b = __uint_as_float((i & 1) ? (bp[n][i >> 1] & 0xFFFF0000u) : (bp[n][i >> 1] << 16));
        v[i] = rbf(g * a) + rbf(omg * b);
      }
      u32x2 o2;
      o2[0] = pk2(v[0], v[1]);
      o2[1] = pk2(v[2], v[3]);
      *(u32x2*)(out + eo + n * 16) = o2;
    }
  }
}

extern "C" void kernel_launch(void* const* d_in, const int* in_sizes, int n_in, void* d_out, int out_size, void* d_ws, size_t ws_size,
                              hipStream_t stream) {
  const u16* x = (const u16*)d_in[0];
  const u16* wqkv = (const u16*)d_in[1];
  const u16* wo = (const u16*)d_in[2];
  const u16* w1 = (const u16*)d_in[4];
  const u16* w2 = (const u16*)d_in[5];
  const u16* gw = (const u16*)d_in[6];
  char* ws = (char*)d_ws;
  hipFuncSetAttribute((const void*)k1_kernel, hipFuncAttributeMaxDynamicSharedMemorySize, K1_LDS);
  hipFuncSetAttribute((const void*)k2_kernel, hipFuncAttributeMaxDynamicSharedMemorySize, K2_LDS);
  hipFuncSetAttribute((const void*)k3_kernel, hipFuncAttributeMaxDynamicSharedMemorySize, K3_LDS);
  k1_kernel<<<232, 512, K1_LDS, stream>>>(x, wqkv, w1, ws);
  k2_kernel<<<256 + 192, 512, K2_LDS, stream>>>(x, w2, gw, ws);
  k3_kernel<<<256, 512, K3_LDS, stream>>>(wo, ws, (u16*)d_out);
}
```

```cpp
#include <hip/hip_runtime.h>
#include <hip/hip_bf16.h>
#include <stdint.h>

typedef short bf16x8 __attribute__((ext_vector_type(8)));
typedef short bf16x4 __attribute__((ext_vector_type(4)));
typedef float f32x16 __attribute__((ext_vector_type(16)));
typedef float f32x4 __attribute__((ext_vector_type(4)));
typedef float f32x2 __attribute__((ext_vector_type(2)));
typedef __bf16 bfx2 __attribute__((ext_vector_type(2)));
typedef unsigned u32x4 __attribute__((ext_vector_type(4)));
typedef unsigned u32x2 __attribute__((ext_vector_type(2)));
typedef __attribute__((address_space(3))) char lds_char;
typedef __attribute__((address_space(1))) unsigned gu32;
typedef __attribute__((address_space(1))) unsigned long long gu64;
typedef unsigned short u16;

#define DI __device__ __forceinline__
#define MFMA16(a, b, c) __builtin_amdgcn_mfma_f32_32x32x16_bf16((a), (b), (c), 0, 0, 0)
#define MFMA8(a, b, c) __builtin_amdgcn_mfma_f32_32x32x8bf16_1k((a), (b), (c), 0, 0, 0)
#define MFMAS(a, b, c) __builtin_amdgcn_mfma_f32_16x16x32_bf16((a), (b), (c), 0, 0, 0)

constexpr int SEQ = 2048, DM = 640, NH = 32, DH = 20, DFF = 1728, NQKV = 1920;

constexpr size_t WS_KV = 0;
constexpr size_t WS_Q = WS_KV + (size_t)32 * 64 * 3072;
constexpr size_t WS_H = WS_Q + (size_t)32 * 64 * 1536;
constexpr size_t WS_O = WS_H + (size_t)64 * 108 * 1024;
constexpr size_t WS_B = WS_O + (size_t)128 * 20 * 1024;
constexpr size_t WS_G = WS_B + (size_t)3 * 2048 * 640 * 4;
constexpr size_t WS_FLAG = WS_G + 2048 * 4;

DI unsigned pk2(float lo, float hi) {
  f32x2 v = {lo, hi};
  bfx2 r = __builtin_convertvector(v, bfx2);
  return __builtin_bit_cast(unsigned, r);
}
DI float rbf(float x) { return (float)(__bf16)x; }
DI float bfbits2f(unsigned short b) { return __uint_as_float((unsigned)b << 16); }

DI void glds16(const char* sbase, unsigned voff, unsigned lds_dst) {
  unsigned keep;
  lds_dst = __builtin_amdgcn_readfirstlane(lds_dst);
  asm volatile("s_mov_b32 %0, m0\n\ts_mov_b32 m0, %3\n\ts_nop 0\n\tglobal_load_lds_dwordx4 %1, %2\n\ts_mov_b32 m0, %0"
               : "=&s"(keep) : "v"(voff), "s"(sbase), "s"(lds_dst) : "memory");
}
typedef short v4i16_t __attribute__((ext_vector_type(4)));
DI bf16x4 tr_read(lds_char* p) {
  return __builtin_bit_cast(bf16x4, __builtin_amdgcn_ds_read_tr16_b64_v4i16((__attribute__((address_space(3))) v4i16_t*)p));
}
#define SBAR() __builtin_amdgcn_sched_barrier(0)
#define PIN(x) asm volatile("" : "+v"(x))
#define WAIT_VM(n) asm volatile("s_waitcnt vmcnt(%0)" ::"n"(n) : "memory")
#define RAW_BAR() do { asm volatile("" ::: "memory"); __builtin_amdgcn_s_waitcnt(0xC07F); __builtin_amdgcn_s_barrier(); asm volatile("" ::: "memory"); } while (0)
#define LDS128(p) (*(__attribute__((address_space(3))) bf16x8*)(p))
#define LDS64(p) (*(__attribute__((address_space(3))) bf16x4*)(p))
#define EX(x) __builtin_amdgcn_exp2f(x)

#define TL(i)
#define DBGSTAMP(off)
#define DBGARG
#define DBGPARAM
#define DBGDECL(o)
#define DBGDUMP(off, KID)
constexpr int DBG_EXTRA = 0;

template <int MT, int NT> struct Frag { bf16x8 a[MT]; bf16x4 bl[NT], bh[NT]; };

template <int MT, int NT, int AMODE, bool SWAP, int NS>
DI void gemm8(f32x16 (&acc)[MT][NT], const char* Abase, unsigned a_rt_stride, const char* Bbase, unsigned ldb_bytes, int cmax, int nk,
              lds_char* lds DBGPARAM) {
  constexpr int BM = 128 * MT, BN = 64 * NT, BK = 64, KS = 4;
  constexpr int ABYTES = BM * BK * 2, BBYTES = BK * BN * 2, STAGE = ABYTES + BBYTES;
  constexpr int APW = ABYTES / 8192, BPW = BBYTES / 8192, NLD = APW + BPW;
  const int tid = threadIdx.x, l = tid & 63;
  const int w = __builtin_amdgcn_readfirstlane(tid >> 6);
  const int wr = w >> 1, wc = w & 1;
  const int r = l & 31, h = l >> 5;
  unsigned aoff[APW];
#pragma unroll
  for (int i = 0; i < APW; ++i) {
    const int pid = APW * w + i;
    if (AMODE == 0) {
      int row = 8 * pid + (l >> 3);
      aoff[i] = row * 1280 + (((l & 7) ^ ((row >> 1) & 7)) * 16);
    } else {
      aoff[i] = (pid / KS) * a_rt_stride + (pid % KS) * 1024 + l * 16;
    }
  }
  constexpr int BROWS = (BN == 128) ? 4 : 8;
  unsigned boff;
  if (BN == 128) {
    int c = (l & 15) ^ ((l >> 4) << 2);
    c = c < cmax ? c : cmax;
    boff = (BROWS * BPW * w + (l >> 4)) * ldb_bytes + c * 16;
  } else {
    int c = (l & 7) ^ (((l >> 4) & 1) << 2);
    c = c < cmax ? c : cmax;
    boff = (BROWS * BPW * w + (l >> 3)) * ldb_bytes + c * 16;
  }
  const unsigned lds0 = (unsigned)(uintptr_t)lds;
  auto piece = [&](int kt, int slot, int i) {
    const unsigned dst = lds0 + slot * STAGE;
    if (i < APW) {
      const char* ap = Abase + (AMODE == 0 ? kt * (BK * 2) : kt * (KS * 1024));
      glds16(ap, aoff[i < APW ? i : 0], dst + (APW * w + i) * 1024);
    } else {
      const char* bp = Bbase + (size_t)kt * BK * ldb_bytes;
      glds16(bp + (i - APW) * BROWS * ldb_bytes, boff, dst + ABYTES + (BPW * w + (i - APW)) * 1024);
    }
  };
  auto issue = [&](int kt, int slot) {
#pragma unroll
    for (int i = 0; i < NLD; ++i) piece(kt, slot, i);
  };
  unsigned ard[KS];
#pragma unroll
  for (int ks = 0; ks < KS; ++ks) {
    if (AMODE == 0) ard[ks] = (wr * (32 * MT) + r) * 128 + (((2 * ks + h) ^ ((r >> 1) & 7)) * 16);
    else ard[ks] = (wr * MT) * 4096 + ks * 1024 + l * 16;
  }
  unsigned brd[NT];
  {
    const int q = (l & 15) >> 2, p = l & 3, cbh = (l >> 4) & 1;
    const int krow = (AMODE == 0 ? 8 * h : 4 * h) + q;
#pragma unroll
    for (int n = 0; n < NT; ++n) {
      int chunk = (wc * NT + n) * 4 + 2 * cbh + (p >> 1);
      if (BN == 128) chunk ^= (q << 2); else chunk ^= (((q >> 1) & 1) << 2);
      brd[n] = ABYTES + krow * (BN * 2) + chunk * 16 + (p & 1) * 8;
    }
  }
  constexpr int TOFF = (AMODE == 0 ? 4 : 8) * (BN * 2);
  auto ldA = [&](Frag<MT, NT>& f, lds_char* sb, int ks, int m) { f.a[m] = LDS128(sb + ard[ks] + m * 4096); };
  auto ldB = [&](Frag<MT, NT>& f, lds_char* sb, int ks, int n) {
    f.bl[n] = tr_read(sb + brd[n] + ks * 16 * (BN * 2));
    f.bh[n] = tr_read(sb + brd[n] + ks * 16 * (BN * 2) + TOFF);
  };
  auto mma = [&](Frag<MT, NT>& f, int m, int n) {
    bf16x8 bb = __builtin_shufflevector(f.bl[n], f.bh[n], 0, 1, 2, 3, 4, 5, 6, 7);
    acc[m][n] = SWAP ? MFMA16(bb, f.a[m], acc[m][n]) : MFMA16(f.a[m], bb, acc[m][n]);
  };
  auto step = [&](Frag<MT, NT>& cur, Frag<MT, NT>& nxt, lds_char* nsb, int nks, bool pf, int pkt, int pslot, int P0, int P1) {
    mma(cur, 0, 0);
    ldB(nxt, nsb, nks, 0); ldA(nxt, nsb, nks, 0);
    SBAR();
    if (NT == 2) { mma(cur, 0, NT - 1); ldB(nxt, nsb, nks, NT - 1); }
    if (MT == 2) ldA(nxt, nsb, nks, MT - 1);
    SBAR();
    if (pf && P0 < P1) piece(pkt, pslot, P0);
    if (MT == 2) { mma(cur, MT - 1, 0); SBAR(); }
    if (pf) { for (int i = P0 + 1; i < P1; ++i) piece(pkt, pslot, i); }
    if (MT == 2 && NT == 2) mma(cur, MT - 1, NT - 1);
    SBAR();
  };
  constexpr int PQ = (NLD + 2) / 3;
  constexpr int P1 = PQ, P2 = 2 * PQ < NLD ? 2 * PQ : NLD;
  DBGSTAMP(DBG_OFF);
  issue(0, 0);
  issue(1, 1);
  Frag<MT, NT> X, Y;
  WAIT_VM(NLD);
  RAW_BAR();
#pragma unroll
  for (int n = 0; n < NT; ++n) ldB(X, lds, 0, n);
#pragma unroll
  for (int m = 0; m < MT; ++m) ldA(X, lds, 0, m);
  int slot = 0, pslot = NS - 1;
  static_assert(NS == 3, "");
#pragma unroll 1
  for (int t = 0; t < nk; ++t) {
    lds_char* sb = lds + slot * STAGE;
    const int pkt = t + NS - 1;
    const bool pf = pkt < nk;
    step(X, Y, sb, 1, pf, pkt, pslot, 0, P1);
    step(Y, X, sb, 2, pf, pkt, pslot, P1, P2);
    step(X, Y, sb, 3, pf, pkt, pslot, P2, NLD);
    if (pf) WAIT_VM(NLD); else WAIT_VM(0);
    RAW_BAR();
    DBGSTAMP(DBG_OFF);
    pslot = slot;
    slot = (slot + 1 == NS) ? 0 : slot + 1;
    step(Y, X, lds + slot * STAGE, 0, false, 0, 0, 0, 0);
  }
  RAW_BAR();
}

constexpr int K1_NS = 3;
constexpr int K1_LDS = K1_NS * 49152 + DBG_EXTRA;
constexpr float QSCALE = 0.22360679774997896f * 1.4426950408889634f;

DI unsigned unit_off(int U, unsigned head_stride) {
  const int p = U / 5, ui = U - p * 5;
  return (2 * p + (ui >= 3 ? 1 : 0)) * head_stride + (ui == 2 ? 1024 : (ui >= 3 ? ui - 3 : ui) * 512);
}

__global__ __launch_bounds__(512, 2) void k1_kernel(const u16* __restrict__ x, const u16* __restrict__ wqkv, const u16* __restrict__ w1,
                                                    const u16* __restrict__ gw, char* __restrict__ ws) {
  extern __shared__ __attribute__((aligned(16))) char smem[];
  lds_char* lds = (lds_char*)smem;
  const int bid = blockIdx.x, tid = threadIdx.x, l = tid & 63;
  const int w = __builtin_amdgcn_readfirstlane(tid >> 6);
  if (bid >= 232) {
    if (bid < 248) {
#pragma unroll
      for (int half = 0; half < 2; ++half) {
        const int row = (bid - 232) * 128 + half * 64 + w * 8 + (l >> 3), sub = l & 7;
        float s = 0.f;
#pragma unroll
        for (int i = 0; i < 10; ++i) {
          const int c = (i * 8 + sub) * 8;
          bf16x8 xv = *(const bf16x8*)(x + (size_t)row * DM + c);
          bf16x8 gv = *(const bf16x8*)(gw + c);
#pragma unroll
          for (int q = 0; q < 8; ++q) s += rbf(bfbits2f((u16)xv[q]) * bfbits2f((u16)gv[q]));
        }
        s += __shfl_xor(s, 1); s += __shfl_xor(s, 2); s += __shfl_xor(s, 4);
        s = rbf(s);
        float g = rbf(1.f / (1.f + __expf(-s)));
        if (sub == 0) ((float*)(ws + WS_G))[row] = g;
      }
    }
    return;
  }
  const int rowp = bid & 7, ct = bid >> 3;
  const int wr = w >> 1, wc = w & 1, r = l & 31, h = l >> 5;
  if (bid == 0 && tid < 24) *(unsigned*)(ws + WS_FLAG + tid * 256) = 0u;
  const char* Abase = (const char*)x + (size_t)rowp * 256 * 1280;
  const int tile0 = rowp * 8 + wr * 2;
  DBGDECL(K1_NS * 49152)
  f32x16 acc[2][2];
#pragma unroll
  for (int m = 0; m < 2; ++m)
#pragma unroll
    for (int n = 0; n < 2; ++n)
#pragma unroll
      for (int i = 0; i < 16; ++i) acc[m][n][i] = 0.f;
  if (ct >= 10 && ct < 15) {
    gemm8<2, 2, 0, false, K1_NS>(acc, Abase, 0, (const char*)wqkv + ct * 256, NQKV * 2, 15, 10, lds DBGARG);
#pragma unroll
    for (int n = 0; n < 2; ++n) {
      const int f = (ct - 10) * 128 + wc * 64 + n * 32 + r;
      const int head = f / 20, d = f - head * 20;
      char* base = ws + WS_KV + (size_t)(head * 64 + tile0) * 3072 + 1536 + h * 384 + d * 16;
#pragma unroll
      for (int m = 0; m < 2; ++m) {
#pragma unroll
        for (int s = 0; s < 2; ++s) {
          u32x4 v;
#pragma unroll
          for (int j = 0; j < 4; ++j) v[j] = pk2(acc[m][n][8 * s + 2 * j], acc[m][n][8 * s + 2 * j + 1]);
          *(u32x4*)(base + m * 3072 + s * 768) = v;
          if (d == 19) { u32x4 o = {0x3F803F80u, 0x3F803F80u, 0x3F803F80u, 0x3F803F80u}; *(u32x4*)(base + m * 3072 + s * 768 + 16) = o; }
        }
      }
    }
  } else {
    const bool isqk = ct < 10;
    const char* Bb = isqk ? (const char*)wqkv + ct * 256 : (const char*)w1 + (ct - 15) * 256;
    gemm8<2, 2, 0, true, K1_NS>(acc, Abase, 0, Bb, isqk ? NQKV * 2 : DFF * 2, ct == 28 ? 7 : 15, 10, lds DBGARG);
    if (isqk) {
      const bool isq = ct < 5;
      const float sc = isq ? QSCALE : 1.f;
      const unsigned ts = isq ? 1536 : 3072;
      char* base = ws + (isq ? WS_Q : WS_KV) + (size_t)tile0 * ts;
#pragma unroll
      for (int n = 0; n < 2; ++n) {
        const int ub = (isq ? ct : ct - 5) * 16 + wc * 8 + n * 4;
#pragma unroll
        for (int gp = 0; gp < 2; ++gp) {
          const int U0 = ub + 2 * gp;
          const unsigned o0 = unit_off(U0, 64 * ts), o1 = unit_off(U0 + 1, 64 * ts);
          const unsigned voff = r * 16 + h * (o1 - o0);
          const bool mixed = ((U0 + h) % 5) == 2;
#pragma unroll
          for (int m = 0; m < 2; ++m) {
            const f32x16& a = acc[m][n];
            unsigned a0 = pk2(a[8 * gp] * sc, a[8 * gp + 1] * sc), a1 = pk2(a[8 * gp + 2] * sc, a[8 * gp + 3] * sc);
            unsigned b0 = pk2(a[8 * gp + 4] * sc, a[8 * gp + 5] * sc), b1 = pk2(a[8 * gp + 6] * sc, a[8 * gp + 7] * sc);
            auto s0 = __builtin_amdgcn_permlane32_swap(a0, b0, false, false);
            auto s1 = __builtin_amdgcn_permlane32_swap(a1, b1, false, false);
            u32x4 v = {s0[0], s1[0], s0[1], s1[1]};
            char* p = base + o0 + m * ts + voff;
            *(u32x4*)p = v;
            if (mixed) *(u32x4*)(p + 64 * ts) = v;
          }
        }
      }
    } else {
#pragma unroll
      for (int n = 0; n < 2; ++n) {
        const int c32 = (ct - 15) * 4 + wc * 2 + n;
        if (c32 < 54) {
#pragma unroll
          for (int m = 0; m < 2; ++m) {
#pragma unroll
            for (int s = 0; s < 2; ++s) {
              u32x4 v;
#pragma unroll
              for (int q = 0; q < 4; ++q) v[q] = pk2(fmaxf(acc[m][n][8 * s + 2 * q], 0.f), fmaxf(acc[m][n][8 * s + 2 * q + 1], 0.f));
              *(u32x4*)(ws + WS_H + ((size_t)((tile0 + m) * 2 + (r >> 4)) * 54 + c32) * 1024 + ((2 * s + h) * 16 + (r & 15)) * 16) = v;
            }
          }
        }
      }
    }
  }
  DBGDUMP(K1_NS * 49152, 1);
}

#define FFN_PRIO 0
constexpr int K2_LDS = 81920;

DI float xhalf_max(float m) {
  auto rr = __builtin_amdgcn_permlane32_swap(__float_as_uint(m), __float_as_uint(m), false, false);
  return fmaxf(__uint_as_float(rr[0]), __uint_as_float(rr[1]));
}
DI float max16(const f32x16& s) {
  float a = fmaxf(fmaxf(s[0], s[1]), s[2]), b = fmaxf(fmaxf(s[3], s[4]), s[5]);
  a = fmaxf(fmaxf(a, s[6]), s[7]); b = fmaxf(fmaxf(b, s[8]), s[9]);
  a = fmaxf(fmaxf(a, s[10]), s[11]); b = fmaxf(fmaxf(b, s[12]), s[13]);
  a = fmaxf(fmaxf(a, s[14]), s[15]);
  return fmaxf(a, b);
}
DI void wait_count(gu32* flag, unsigned target) {
  for (unsigned spins = 0; spins < (1u << 18); ++spins) {
    if ((unsigned)__builtin_amdgcn_readfirstlane(__hip_atomic_load(flag, __ATOMIC_RELAXED, __HIP_MEMORY_SCOPE_AGENT)) >= target) break;
    __builtin_amdgcn_s_sleep(2);
  }
}

DI void attn_wg(char* __restrict__ ws, lds_char* lds, int bid) {
  const int tid = threadIdx.x, l = tid & 63;
  const int w = __builtin_amdgcn_readfirstlane(tid >> 6);
  const int r = l & 31, h = l >> 5;
  const int xcd = bid & 7, idx = bid >> 3;
  const int head = xcd * 4 + (idx >> 3), qb = idx & 7;
  const int qt = qb * 8 + w;
  const char* kv = ws + WS_KV + (size_t)head * 64 * 3072;
  const unsigned lds0 = (unsigned)(uintptr_t)lds;
  const char* kvp = kv + (w < 6 ? w : 0) * 1024;
  const unsigned kvdst = lds0 + (w < 6 ? w * 1024 : 24576), kvstep = w < 6 ? 6144 : 0;
  auto dma_kv = [&](int ch, int U) { glds16(kvp + (size_t)ch * 6144, l * 16, kvdst + U * kvstep); };
  TL(0);
  const char* qp = ws + WS_Q + (size_t)(head * 64 + qt) * 1536;
  bf16x8 q1;
  u32x2 q2u;
  asm volatile("global_load_dwordx4 %0, %1, %2" : "=v"(q1) : "v"(l * 16), "s"(qp) : "memory");
  asm volatile("global_load_dwordx2 %0, %1, %2" : "=v"(q2u) : "v"(1024 + r * 16 + (head & 1) * 8), "s"(qp) : "memory");
  dma_kv(0, 0); dma_kv(1, 1); dma_kv(2, 2);
  const unsigned k1rd = l * 16, k2rd = h ? 1536 + 20 * 16 : 1024 + r * 16 + (head & 1) * 8;
  const int dd = r < 24 ? r : (r < 28 ? r - 4 : r - 12);
  const unsigned vrd = 1536 + h * 384 + dd * 16;
  f32x16 zero;
#pragma unroll
  for (int i = 0; i < 16; ++i) zero[i] = 0.f;
  asm volatile("s_waitcnt vmcnt(2)" : "+v"(q1), "+v"(q2u)::"memory");
  RAW_BAR();
  if (h) { q2u[0] = 0; q2u[1] = 0; }
  bf16x8 kf1 = LDS128(lds + k1rd);
  bf16x4 kf2 = LDS64(lds + k2rd);
  {
    f32x16 s = MFMA16(kf1, q1, zero);
    s = MFMA8(kf2, __builtin_bit_cast(bf16x4, q2u), s);
    float m = xhalf_max(max16(s));
    if (h) q2u[0] = pk2(-m, 0.f);
  }
  const bf16x4 q2 = __builtin_bit_cast(bf16x4, q2u);
  f32x16 o = zero;
  f32x16 sA = MFMA16(kf1, q1, zero);
  sA = MFMA8(kf2, q2, sA);
  f32x16 sB;
  kf1 = LDS128(lds + 3072 + k1rd);
  kf2 = LDS64(lds + 3072 + k2rd);
  bf16x8 vf0 = LDS128(lds + vrd);
  bf16x8 vf1 = vf0;
  u32x4 p0, p1 = {0u, 0u, 0u, 0u};
#define AT_STEP(SC, SN, KN, VC, VN, D0) do { \
    o = MFMA16(vf1, __builtin_bit_cast(bf16x8, p1), o); PIN(o); \
    vf1 = LDS128((VC) + vrd + 768); SBAR(); \
    SC[0] = EX(SC[0]); SC[1] = EX(SC[1]); SC[2] = EX(SC[2]); SC[3] = EX(SC[3]); PIN(SC); SBAR(); \
    SN = MFMA16(kf1, q1, zero); \
    kf1 = LDS128((KN) + k1rd); SBAR(); \
    D0; \
    SC[4] = EX(SC[4]); SC[5] = EX(SC[5]); SC[6] = EX(SC[6]); SC[7] = EX(SC[7]); \
    p0[0] = pk2(SC[0], SC[1]); p0[1] = pk2(SC[2], SC[3]); p0[2] = pk2(SC[4], SC[5]); p0[3] = pk2(SC[6], SC[7]); PIN(p0); SBAR(); \
    o = MFMA16(vf0, __builtin_bit_cast(bf16x8, p0), o); PIN(o); \
    vf0 = LDS128((VN) + vrd); SBAR(); \
    SC[8] = EX(SC[8]); SC[9] = EX(SC[9]); SC[10] = EX(SC[10]); SC[11] = EX(SC[11]); PIN(SC); SBAR(); \
    SN = MFMA8(kf2, q2, SN); \
    kf2 = LDS64((KN) + k2rd); SBAR(); \
    SC[12] = EX(SC[12]); SC[13] = EX(SC[13]); SC[14] = EX(SC[14]); SC[15] = EX(SC[15]); \
    p1[0] = pk2(SC[8], SC[9]); p1[1] = pk2(SC[10], SC[11]); p1[2] = pk2(SC[12], SC[13]); p1[3] = pk2(SC[14], SC[15]); PIN(p1); SBAR(); \
  } while (0)
#define NOP_ ((void)0)
#define SUPER(U, WN, X0) do { \
    WAIT_VM(WN); \
    RAW_BAR(); \
    AT_STEP(sA, sB, lds + (((U) + 1) & 3) * 6144, lds + (U) * 6144, lds + (U) * 6144 + 3072, X0); \
    AT_STEP(sB, sA, lds + (((U) + 1) & 3) * 6144 + 3072, lds + (U) * 6144 + 3072, lds + (((U) + 1) & 3) * 6144, NOP_); \
  } while (0)
  TL(1);
#pragma unroll 1
  for (int c = 0; c < 28; c += 4) {
    SUPER(0, 1, dma_kv(c + 3, 3)); SUPER(1, 1, dma_kv(c + 4, 0)); SUPER(2, 1, dma_kv(c + 5, 1)); SUPER(3, 1, dma_kv(c + 6, 2));
  }
  SUPER(0, 1, dma_kv(31, 3)); SUPER(1, 1, NOP_); SUPER(2, 0, NOP_); SUPER(3, 0, NOP_);
  o = MFMA16(vf1, __builtin_bit_cast(bf16x8, p1), o);
  TL(2);
  float lsum;
  {
    auto rr = __builtin_amdgcn_permlane32_swap(__float_as_uint(o[8]), __float_as_uint(o[8]), false, false);
    lsum = __uint_as_float(rr[1]);
  }
  bool okv = lsum < 1.0e18f;
#pragma unroll
  for (int i = 0; i < 12; ++i) okv = okv && ((h && i >= 8) ? true : fabsf(o[i]) < 1.0e30f);
  if (!__all(okv)) {
    u32x2 qz = q2u; if (h) qz[0] = 0;
    const bf16x4 q2z = __builtin_bit_cast(bf16x4, qz);
    float m = -INFINITY;
    o = zero;
    for (int t = 0; t < 64; ++t) {
      const char* tb = kv + (size_t)t * 3072;
      bf16x8 k1 = *(const bf16x8*)(tb + k1rd);
      bf16x4 k2 = *(const bf16x4*)(tb + k2rd);
      bf16x8 v0 = *(const bf16x8*)(tb + vrd);
      bf16x8 v1 = *(const bf16x8*)(tb + vrd + 768);
      f32x16 s = MFMA16(k1, q1, zero);
      s = MFMA8(k2, q2z, s);
      float mn = fmaxf(m, xhalf_max(max16(s)));
      float alpha = EX(m - mn);
      m = mn;
#pragma unroll
      for (int i = 0; i < 16; ++i) o[i] *= alpha;
      u32x4 a0, a1;
#pragma unroll
      for (int j = 0; j < 4; ++j) {
        a0[j] = pk2(EX(s[2 * j] - m), EX(s[2 * j + 1] - m));
        a1[j] = pk2(EX(s[8 + 2 * j] - m), EX(s[8 + 2 * j + 1] - m));
      }
      o = MFMA16(v0, __builtin_bit_cast(bf16x8, a0), o);
      o = MFMA16(v1, __builtin_bit_cast(bf16x8, a1), o);
    }
    auto rr = __builtin_amdgcn_permlane32_swap(__float_as_uint(o[8]), __float_as_uint(o[8]), false, false);
    lsum = __uint_as_float(rr[1]);
  }
  const float inv = 1.f / lsum;
  char* ob = ws + WS_O + (size_t)(qt * 2 + (r >> 4)) * 20 * 1024 + (r & 15) * 16;
#pragma unroll
  for (int g = 0; g < 3; ++g) {
    const int gi = 2 * g + h;
    if (gi < 5) {
      const int G = 5 * head + gi;
      u32x2 v;
      v[0] = pk2(o[4 * g] * inv, o[4 * g + 1] * inv);
      v[1] = pk2(o[4 * g + 2] * inv, o[4 * g + 3] * inv);
      __hip_atomic_store((gu64*)(ob + (G >> 3) * 1024 + (G & 3) * 256 + ((G >> 2) & 1) * 8), __builtin_bit_cast(unsigned long long, v),
                         __ATOMIC_RELAXED, __HIP_MEMORY_SCOPE_AGENT);
    }
  }
  asm volatile("s_waitcnt vmcnt(0)" ::: "memory");
  RAW_BAR();
  if (tid == 0) __hip_atomic_fetch_add((gu32*)(ws + WS_FLAG + qb * 256), 1u, __ATOMIC_RELAXED, __HIP_MEMORY_SCOPE_AGENT);
  TL(3);
}

DI void out_wg(const u16* __restrict__ w2, const u16* __restrict__ wo, char* __restrict__ ws, u16* __restrict__ out, lds_char* lds, int j) {
  const int tid = threadIdx.x, l = tid & 63;
  const int w = __builtin_amdgcn_readfirstlane(tid >> 6);
  const unsigned lds0 = (unsigned)(uintptr_t)lds;
  const int wt = w & 3, kh = w >> 2;
  const int rt = j & 31, ct = j >> 5;
  const int tk = l & 15, kq = l >> 4;
  const int token = rt * 64 + wt * 16 + tk;
  lds_char* red = lds + wt * 5120 + l * 16;
  TL(0);
  float bt[3][4];
  const float g = ((const float*)(ws + WS_G))[token];
  {
    unsigned poff[4];
#pragma unroll
    for (int i = 0; i < 4; ++i) {
      const int pc = i * 8 + w, ci = (pc < 30 ? pc : 0) * 64 + l, row = ci / 10, c = ci - row * 10;
      poff[i] = row * 1280 + c * 16;
    }
    const char* wb2 = (const char*)w2 + ct * 160;
    auto wstage = [&](int st, int slot) {
#pragma unroll
      for (int i = 0; i < 4; ++i) {
        const int pc = i * 8 + w;
        glds16(wb2 + (size_t)st * 192 * 1280, poff[i], lds0 + (pc < 30 ? slot * 30720 + pc * 1024 : 61440 + (pc - 30) * 1024));
      }
    };
    const bf16x8* hp = (const bf16x8*)(ws + WS_H + (size_t)((rt * 4 + wt) * 54 + kh * 3) * 1024) + l;
    bf16x8 hn[3], hc[3];
#pragma unroll
    for (int i = 0; i < 3; ++i) hn[i] = hp[i * 64];
    wstage(0, 0);
    f32x4 accb[5];
#pragma unroll
    for (int n = 0; n < 5; ++n) accb[n] = f32x4{0.f, 0.f, 0.f, 0.f};
    const unsigned br2 = (kh * 96 + 16 * (kq >> 1) + 4 * (kq & 1) + (tk >> 2)) * 160 + (tk & 3) * 8;
#pragma unroll 1
    for (int st = 0; st < 9; ++st) {
      WAIT_VM(0);
      RAW_BAR();
#pragma unroll
      for (int i = 0; i < 3; ++i) hc[i] = hn[i];
      if (st + 1 < 9) {
#pragma unroll
        for (int i = 0; i < 3; ++i) hn[i] = hp[((st + 1) * 6 + i) * 64];
        wstage(st + 1, (st + 1) & 1);
      }
      lds_char* sb = lds + (st & 1) * 30720 + br2;
#pragma unroll
      for (int i = 0; i < 3; ++i)
#pragma unroll
        for (int n = 0; n < 5; ++n) {
          bf16x4 lo = tr_read(sb + i * 32 * 160 + n * 32);
          bf16x4 hi = tr_read(sb + (i * 32 + 8) * 160 + n * 32);
          accb[n] = MFMAS(__builtin_shufflevector(lo, hi, 0, 1, 2, 3, 4, 5, 6, 7), hc[i], accb[n]);
        }
    }
    __builtin_amdgcn_s_setprio(1);
    RAW_BAR();
#pragma unroll
    for (int n = 0; n < 5; ++n)
      if ((n < 3) != (kh == 0)) *(__attribute__((address_space(3))) f32x4*)(red + n * 1024) = accb[n];
    RAW_BAR();
    const float omg = rbf(1.f - g);
#pragma unroll
    for (int n = 0; n < 5; ++n)
      if ((n < 3) == (kh == 0)) {
        f32x4 a2 = *(__attribute__((address_space(3))) f32x4*)(red + n * 1024);
#pragma unroll
        for (int i = 0; i < 4; ++i) bt[n % 3][i] = rbf(omg * rbf(accb[n][i] + a2[i]));
      }
    RAW_BAR();
  }
  TL(1);
  const char* wb = (const char*)wo + ct * 160;
#pragma unroll
  for (int i = 0; i < 3; ++i) {
    const int pc = i * 8 + w;
    if (pc < 20) {
      const int ci = pc * 64 + l, row = ci / 10, c = ci - row * 10;
      glds16(wb, (row < 64 ? row : row + 256) * 1280 + c * 16, lds0 + pc * 1024);
    }
  }
  WAIT_VM(0);
  RAW_BAR();
  const unsigned brl = (4 * kq + (tk >> 2)) * 160 + (tk & 3) * 8;
  bf16x8 wf[2][5];
#pragma unroll
  for (int ks = 0; ks < 2; ++ks)
#pragma unroll
    for (int n = 0; n < 5; ++n) {
      bf16x4 lo = tr_read(lds + brl + (kh * 64 + ks * 32) * 160 + n * 32);
      bf16x4 hi = tr_read(lds + brl + (kh * 64 + ks * 32 + 16) * 160 + n * 32);
      wf[ks][n] = __builtin_shufflevector(lo, hi, 0, 1, 2, 3, 4, 5, 6, 7);
    }
  RAW_BAR();
#pragma unroll
  for (int i = 0; i < 10; ++i) {
    const int pc = i * 8 + w;
    const int ci = pc * 64 + l, row = ci / 10, c = ci - row * 10;
    glds16(wb, (row < 256 ? row + 64 : row + 128) * 1280 + c * 16, lds0 + pc * 1024);
  }
  TL(2);
  if (w == 0) wait_count((gu32*)(ws + WS_FLAG + (rt >> 2) * 256), 32u);
  WAIT_VM(0);
  RAW_BAR();
  __builtin_amdgcn_s_setprio(3);
  TL(3);
  const char* op = ws + WS_O + (size_t)((rt * 4 + wt) * 20 + kh * 10) * 1024;
  bf16x8 of[10];
#pragma unroll
  for (int ks = 0; ks < 10; ++ks) asm volatile("global_load_dwordx4 %0, %1, %2 sc1" : "=v"(of[ks]) : "v"(l * 16 + ks * 1024), "s"(op) : "memory");
  f32x4 acc[5];
#pragma unroll
  for (int n = 0; n < 5; ++n) acc[n] = f32x4{0.f, 0.f, 0.f, 0.f};
  asm volatile("s_waitcnt vmcnt(0)" : "+v"(of[0]), "+v"(of[1]), "+v"(of[2]), "+v"(of[3]), "+v"(of[4]), "+v"(of[5]), "+v"(of[6]), "+v"(of[7]), "+v"(of[8]), "+v"(of[9])::"memory");
  TL(4);
#pragma unroll
  for (int ks = 0; ks < 10; ++ks) {
#pragma unroll
    for (int n = 0; n < 5; ++n) acc[n] = MFMAS(wf[ks & 1][n], of[ks], acc[n]);
    if (ks + 2 < 10) {
#pragma unroll
      for (int n = 0; n < 5; ++n) {
        bf16x4 lo = tr_read(lds + brl + (kh * 256 + ks * 32) * 160 + n * 32);
        bf16x4 hi = tr_read(lds + brl + (kh * 256 + ks * 32 + 16) * 160 + n * 32);
        wf[ks & 1][n] = __builtin_shufflevector(lo, hi, 0, 1, 2, 3, 4, 5, 6, 7);
      }
    }
    SBAR();
  }
  RAW_BAR();
  TL(5);
#pragma unroll
  for (int n = 0; n < 5; ++n)
    if ((n < 3) != (kh == 0)) *(__attribute__((address_space(3))) f32x4*)(red + n * 1024) = acc[n];
  RAW_BAR();
  const size_t eo = (size_t)token * DM + ct * 80 + 4 * kq;
#pragma unroll
  for (int n = 0; n < 5; ++n) {
    if ((n < 3) == (kh == 0)) {
      f32x4 a2 = *(__attribute__((address_space(3))) f32x4*)(red + n * 1024);
      float v[4];
#pragma unroll
      for (int i = 0; i < 4; ++i) v[i] = rbf(g * rbf(acc[n][i] + a2[i])) + bt[n % 3][i];
      u32x2 o2;
      o2[0] = pk2(v[0], v[1]);
      o2[1] = pk2(v[2], v[3]);
      *(u32x2*)(out + eo + n * 16) = o2;
    }
  }
  TL(6);
}

__global__ __launch_bounds__(512, 4) void k2_kernel(const u16* __restrict__ w2, const u16* __restrict__ wo, char* __restrict__ ws,
                                                    u16* __restrict__ out) {
  extern __shared__ __attribute__((aligned(16))) char smem[];
  lds_char* lds = (lds_char*)smem;
  const int bid = blockIdx.x;
  if (bid < 256) attn_wg(ws, lds, bid);
  else { __builtin_amdgcn_s_setprio(FFN_PRIO); out_wg(w2, wo, ws, out, lds, bid - 256); }
}

extern "C" void kernel_launch(void* const* d_in, const int* in_sizes, int n_in, void* d_out, int out_size, void* d_ws, size_t ws_size,
                              hipStream_t stream) {
  const u16* x = (const u16*)d_in[0];
  const u16* wqkv = (const u16*)d_in[1];
  const u16* wo = (const u16*)d_in[2];
  const u16* w1 = (const u16*)d_in[4];
  const u16* w2 = (const u16*)d_in[5];
  const u16* gw = (const u16*)d_in[6];
  char* ws = (char*)d_ws;
  hipFuncSetAttribute((const void*)k1_kernel, hipFuncAttributeMaxDynamicSharedMemorySize, K1_LDS);
  hipFuncSetAttribute((const void*)k2_kernel, hipFuncAttributeMaxDynamicSharedMemorySize, K2_LDS);
  k1_kernel<<<248, 512, K1_LDS, stream>>>(x, wqkv, w1, gw, ws);
  k2_kernel<<<512, 512, K2_LDS, stream>>>(w2, wo, ws, (u16*)d_out);
}
```

```cpp
#include <hip/hip_runtime.h>
#include <hip/hip_bf16.h>
#include <stdint.h>
#include <type_traits>

typedef short bf16x8 __attribute__((ext_vector_type(8)));
typedef short bf16x4 __attribute__((ext_vector_type(4)));
typedef float f32x16 __attribute__((ext_vector_type(16)));
typedef float f32x4 __attribute__((ext_vector_type(4)));
typedef float f32x2 __attribute__((ext_vector_type(2)));
typedef __bf16 bfx2 __attribute__((ext_vector_type(2)));
typedef unsigned u32x4 __attribute__((ext_vector_type(4)));
typedef unsigned u32x2 __attribute__((ext_vector_type(2)));
typedef __attribute__((address_space(3))) char lds_char;
typedef __attribute__((address_space(1))) unsigned gu32;
typedef __attribute__((address_space(1))) unsigned long long gu64;
typedef unsigned short u16;

#define DI __device__ __forceinline__
#define MFMA16(a, b, c) __builtin_amdgcn_mfma_f32_32x32x16_bf16((a), (b), (c), 0, 0, 0)
#define MFMA8(a, b, c) __builtin_amdgcn_mfma_f32_32x32x8bf16_1k((a), (b), (c), 0, 0, 0)
#define MFMAS(a, b, c) __builtin_amdgcn_mfma_f32_16x16x32_bf16((a), (b), (c), 0, 0, 0)

constexpr int SEQ = 2048, DM = 640, NH = 32, DH = 20, DFF = 1728, NQKV = 1920;

constexpr size_t WS_KV = 0;
constexpr size_t WS_Q = WS_KV + (size_t)32 * 64 * 3072;
constexpr size_t WS_H = WS_Q + (size_t)32 * 64 * 1536;
constexpr size_t WS_O = WS_H + (size_t)64 * 108 * 1024;
constexpr size_t WS_B = WS_O + (size_t)128 * 20 * 1024;
constexpr size_t WS_G = WS_B + (size_t)3 * 2048 * 640 * 4;
constexpr size_t WS_FLAG = WS_G + 2048 * 4;

DI unsigned pk2(float lo, float hi) {
  f32x2 v = {lo, hi};
  bfx2 r = __builtin_convertvector(v, bfx2);
  return __builtin_bit_cast(unsigned, r);
}
#define K1ST 1
DI void st16(void* p, u32x4 v) {
  __builtin_nontemporal_store(v, (u32x4*)p);
}
DI unsigned relu_pk(unsigned v) {
  typedef short s16x2 __attribute__((ext_vector_type(2)));
  s16x2 a = __builtin_bit_cast(s16x2, v), z = {0, 0};
  return __builtin_bit_cast(unsigned, __builtin_elementwise_max(a, z));
}
DI float rbf(float x) { return (float)(__bf16)x; }
DI float bfbits2f(unsigned short b) { return __uint_as_float((unsigned)b << 16); }

DI void glds16(const char* sbase, unsigned voff, unsigned lds_dst) {
  unsigned keep;
  lds_dst = __builtin_amdgcn_readfirstlane(lds_dst);
  asm volatile("s_mov_b32 %0, m0\n\ts_mov_b32 m0, %3\n\ts_nop 0\n\tglobal_load_lds_dwordx4 %1, %2\n\ts_mov_b32 m0, %0"
               : "=&s"(keep) : "v"(voff), "s"(sbase), "s"(lds_dst) : "memory");
}
DI void glds16m(const char* sbase, unsigned voff, unsigned lds_dst, unsigned long long mask) {
  unsigned keep;
  lds_dst = __builtin_amdgcn_readfirstlane(lds_dst);
  asm volatile("s_mov_b32 %0, m0\n\ts_mov_b32 m0, %3\n\ts_mov_b64 exec, %4\n\tglobal_load_lds_dwordx4 %1, %2\n\ts_mov_b64 exec, -1\n\ts_mov_b32 m0, %0"
               : "=&s"(keep) : "v"(voff), "s"(sbase), "s"(lds_dst), "s"(mask) : "memory");
}
typedef short v4i16_t __attribute__((ext_vector_type(4)));
DI bf16x4 tr_read(lds_char* p) {
  return __builtin_bit_cast(bf16x4, __builtin_amdgcn_ds_read_tr16_b64_v4i16((__attribute__((address_space(3))) v4i16_t*)p));
}
#define SBAR() __builtin_amdgcn_sched_barrier(0)
#define PIN(x) asm volatile("" : "+v"(x))
#define WAIT_VM(n) asm volatile("s_waitcnt vmcnt(%0)" ::"n"(n) : "memory")
#define RAW_BAR() do { asm volatile("" ::: "memory"); __builtin_amdgcn_s_waitcnt(0xC07F); __builtin_amdgcn_s_barrier(); asm volatile("" ::: "memory"); } while (0)
#define LDS128(p) (*(__attribute__((address_space(3))) bf16x8*)(p))
#define LDS64(p) (*(__attribute__((address_space(3))) bf16x4*)(p))
#define EX(x) __builtin_amdgcn_exp2f(x)

#define TL(i)
#define DBGSTAMP(off)
#define DBGARG
#define DBGPARAM
#define DBGDECL(o)
#define DBGDUMP(off, KID)
constexpr int DBG_EXTRA = 0;

template <int MT, int NT> struct Frag { bf16x8 a[MT]; bf16x4 bl[NT], bh[NT]; };

template <int MT, int NT, int AMODE, bool SWAP, int NS>
DI void gemm8(f32x16 (&acc)[MT][NT], const char* Abase, unsigned a_rt_stride, const char* Bbase, unsigned ldb_bytes, int cmax, int nk,
              lds_char* lds DBGPARAM) {
  constexpr int BM = 128 * MT, BN = 64 * NT, BK = 64, KS = 4;
  constexpr int ABYTES = BM * BK * 2, BBYTES = BK * BN * 2, STAGE = ABYTES + BBYTES;
  constexpr int APW = ABYTES / 8192, BPW = BBYTES / 8192, NLD = APW + BPW;
  const int tid = threadIdx.x, l = tid & 63;
  const int w = __builtin_amdgcn_readfirstlane(tid >> 6);
  const int wr = w >> 1, wc = w & 1;
  const int r = l & 31, h = l >> 5;
  unsigned aoff[APW];
#pragma unroll
  for (int i = 0; i < APW; ++i) {
    const int pid = APW * w + i;
    if (AMODE == 0) {
      int row = 8 * pid + (l >> 3);
      aoff[i] = row * 1280 + (((l & 7) ^ ((row >> 1) & 7)) * 16);
    } else {
      aoff[i] = (pid / KS) * a_rt_stride + (pid % KS) * 1024 + l * 16;
    }
  }
  constexpr int BROWS = (BN == 128) ? 4 : 8;
  unsigned boff;
  if (BN == 128) {
    int c = (l & 15) ^ ((l >> 4) << 2);
    c = c < cmax ? c : cmax;
    boff = (BROWS * BPW * w + (l >> 4)) * ldb_bytes + c * 16;
  } else {
    int c = (l & 7) ^ (((l >> 4) & 1) << 2);
    c = c < cmax ? c : cmax;
    boff = (BROWS * BPW * w + (l >> 3)) * ldb_bytes + c * 16;
  }
  const unsigned lds0 = (unsigned)(uintptr_t)lds;
  auto piece = [&](int kt, int slot, int i) {
    const unsigned dst = lds0 + slot * STAGE;
    if (i < APW) {
      const char* ap = Abase + (AMODE == 0 ? kt * (BK * 2) : kt * (KS * 1024));
      glds16(ap, aoff[i < APW ? i : 0], dst + (APW * w + i) * 1024);
    } else {
      const char* bp = Bbase + (size_t)kt * BK * ldb_bytes;
      glds16(bp + (i - APW) * BROWS * ldb_bytes, boff, dst + ABYTES + (BPW * w + (i - APW)) * 1024);
    }
  };
  auto issue = [&](int kt, int slot) {
#pragma unroll
    for (int i = 0; i < NLD; ++i) piece(kt, slot, i);
  };
  unsigned ard[KS];
#pragma unroll
  for (int ks = 0; ks < KS; ++ks) {
    if (AMODE == 0) ard[ks] = (wr * (32 * MT) + r) * 128 + (((2 * ks + h) ^ ((r >> 1) & 7)) * 16);
    else ard[ks] = (wr * MT) * 4096 + ks * 1024 + l * 16;
  }
  unsigned brd[NT];
  {
    const int q = (l & 15) >> 2, p = l & 3, cbh = (l >> 4) & 1;
    const int krow = (AMODE == 0 ? 8 * h : 4 * h) + q;
#pragma unroll
    for (int n = 0; n < NT; ++n) {
      int chunk = (wc * NT + n) * 4 + 2 * cbh + (p >> 1);
      if (BN == 128) chunk ^= (q << 2); else chunk ^= (((q >> 1) & 1) << 2);
      brd[n] = ABYTES + krow * (BN * 2) + chunk * 16 + (p & 1) * 8;
    }
  }
  constexpr int TOFF = (AMODE == 0 ? 4 : 8) * (BN * 2);
  auto ldA = [&](Frag<MT, NT>& f, lds_char* sb, int ks, int m) { f.a[m] = LDS128(sb + ard[ks] + m * 4096); };
  auto ldB = [&](Frag<MT, NT>& f, lds_char* sb, int ks, int n) {
    f.bl[n] = tr_read(sb + brd[n] + ks * 16 * (BN * 2));
    f.bh[n] = tr_read(sb + brd[n] + ks * 16 * (BN * 2) + TOFF);
  };
  auto mma = [&](Frag<MT, NT>& f, int m, int n) {
    bf16x8 bb = __builtin_shufflevector(f.bl[n], f.bh[n], 0, 1, 2, 3, 4, 5, 6, 7);
    acc[m][n] = SWAP ? MFMA16(bb, f.a[m], acc[m][n]) : MFMA16(f.a[m], bb, acc[m][n]);
  };
  auto step = [&](Frag<MT, NT>& cur, Frag<MT, NT>& nxt, lds_char* nsb, int nks, bool pf, int pkt, int pslot, int P0, int P1) {
    mma(cur, 0, 0);
    ldB(nxt, nsb, nks, 0); ldA(nxt, nsb, nks, 0);
    SBAR();
    if (NT == 2) { mma(cur, 0, NT - 1); ldB(nxt, nsb, nks, NT - 1); }
    if (MT == 2) ldA(nxt, nsb, nks, MT - 1);
    SBAR();
    if (pf && P0 < P1) piece(pkt, pslot, P0);
    if (MT == 2) { mma(cur, MT - 1, 0); SBAR(); }
    if (pf) { for (int i = P0 + 1; i < P1; ++i) piece(pkt, pslot, i); }
    if (MT == 2 && NT == 2) mma(cur, MT - 1, NT - 1);
    SBAR();
  };
  constexpr int PQ = (NLD + 2) / 3;
  constexpr int P1 = PQ, P2 = 2 * PQ < NLD ? 2 * PQ : NLD;
  DBGSTAMP(DBG_OFF);
  issue(0, 0);
  issue(1, 1);
  Frag<MT, NT> X, Y;
  WAIT_VM(NLD);
  RAW_BAR();
#pragma unroll
  for (int n = 0; n < NT; ++n) ldB(X, lds, 0, n);
#pragma unroll
  for (int m = 0; m < MT; ++m) ldA(X, lds, 0, m);
  int slot = 0, pslot = NS - 1;
  static_assert(NS == 3, "");
  auto body = [&](int t, auto pf_c) {
    constexpr bool pf = decltype(pf_c)::value;
    lds_char* sb = lds + slot * STAGE;
    const int pkt = t + NS - 1;
    step(X, Y, sb, 1, pf, pkt, pslot, 0, P1);
    step(Y, X, sb, 2, pf, pkt, pslot, P1, P2);
    step(X, Y, sb, 3, pf, pkt, pslot, P2, NLD);
    if (pf) WAIT_VM(NLD); else WAIT_VM(0);
    RAW_BAR();
    DBGSTAMP(DBG_OFF);
    pslot = slot;
    slot = (slot + 1 == NS) ? 0 : slot + 1;
    step(Y, X, lds + slot * STAGE, 0, false, 0, 0, 0, 0);
  };
#pragma unroll 1
  for (int t = 0; t < nk - 2; ++t) body(t, std::true_type{});
#pragma unroll 1
  for (int t = 0; t < 2; ++t) body(t, std::false_type{});
  RAW_BAR();
}

constexpr int K1_NS = 3;
constexpr int K1_LDS = K1_NS * 49152 + DBG_EXTRA;
constexpr float QSCALE = 0.22360679774997896f * 1.4426950408889634f;

DI unsigned unit_off(int U, unsigned head_stride) {
  const int p = U / 5, ui = U - p * 5;
  return (2 * p + (ui >= 3 ? 1 : 0)) * head_stride + (ui == 2 ? 1024 : (ui >= 3 ? ui - 3 : ui) * 512);
}

__global__ __launch_bounds__(512, 2) void k1_kernel(const u16* __restrict__ x, const u16* __restrict__ wqkv, const u16* __restrict__ w1,
                                                    const u16* __restrict__ gw, char* __restrict__ ws) {
  extern __shared__ __attribute__((aligned(16))) char smem[];
  lds_char* lds = (lds_char*)smem;
  const int bid = blockIdx.x, tid = threadIdx.x, l = tid & 63;
  const int w = __builtin_amdgcn_readfirstlane(tid >> 6);
  if (bid >= 232) {
    if (bid < 248) {
#pragma unroll
      for (int half = 0; half < 2; ++half) {
        const int row = (bid - 232) * 128 + half * 64 + w * 8 + (l >> 3), sub = l & 7;
        float s = 0.f;
#pragma unroll
        for (int i = 0; i < 10; ++i) {
          const int c = (i * 8 + sub) * 8;
          bf16x8 xv = *(const bf16x8*)(x + (size_t)row * DM + c);
          bf16x8 gv = *(const bf16x8*)(gw + c);
#pragma unroll
          for (int q = 0; q < 8; ++q) s += rbf(bfbits2f((u16)xv[q]) * bfbits2f((u16)gv[q]));
        }
        s += __shfl_xor(s, 1); s += __shfl_xor(s, 2); s += __shfl_xor(s, 4);
        s = rbf(s);
        float g = rbf(1.f / (1.f + __expf(-s)));
        if (sub == 0) ((float*)(ws + WS_G))[row] = g;
      }
    } else {
      const u32x4 sn = {0xFFFFFFFFu, 0xFFFFFFFFu, 0xFFFFFFFFu, 0xFFFFFFFFu};
      char* p = ws + WS_O + (size_t)(bid - 248) * 327680 + tid * 16;
#pragma unroll 8
      for (int i = 0; i < 40; ++i) st16(p + i * 8192, sn);
    }
    return;
  }
  const int rowp = bid & 7, ct = bid >> 3;
  const int wr = w >> 1, wc = w & 1, r = l & 31, h = l >> 5;
  if (bid == 0 && tid < 24) *(unsigned*)(ws + WS_FLAG + tid * 256) = 0u;
  const char* Abase = (const char*)x + (size_t)rowp * 256 * 1280;
  const int tile0 = rowp * 8 + wr * 2;
  DBGDECL(K1_NS * 49152)
  f32x16 acc[2][2];
#pragma unroll
  for (int m = 0; m < 2; ++m)
#pragma unroll
    for (int n = 0; n < 2; ++n)
#pragma unroll
      for (int i = 0; i < 16; ++i) acc[m][n][i] = 0.f;
  if (ct >= 10 && ct < 15) {
    gemm8<2, 2, 0, false, K1_NS>(acc, Abase, 0, (const char*)wqkv + ct * 256, NQKV * 2, 15, 10, lds DBGARG);
#pragma unroll
    for (int n = 0; n < 2; ++n) {
      const int f = (ct - 10) * 128 + wc * 64 + n * 32 + r;
      const int head = f / 20, d = f - head * 20;
      char* base = ws + WS_KV + (size_t)(head * 64 + tile0) * 3072 + 1536 + h * 384 + d * 16;
#pragma unroll
      for (int m = 0; m < 2; ++m) {
#pragma unroll
        for (int s = 0; s < 2; ++s) {
          u32x4 v;
#pragma unroll
          for (int j = 0; j < 4; ++j) v[j] = pk2(acc[m][n][8 * s + 2 * j], acc[m][n][8 * s + 2 * j + 1]);
          st16(base + m * 3072 + s * 768, v);
          if (d == 19) { u32x4 o = {0x3F803F80u, 0x3F803F80u, 0x3F803F80u, 0x3F803F80u}; st16(base + m * 3072 + s * 768 + 16, o); }
        }
      }
    }
  } else {
    const bool isqk = ct < 10;
    const char* Bb = isqk ? (const char*)wqkv + ct * 256 : (const char*)w1 + (ct - 15) * 256;
    gemm8<2, 2, 0, true, K1_NS>(acc, Abase, 0, Bb, isqk ? NQKV * 2 : DFF * 2, ct == 28 ? 7 : 15, 10, lds DBGARG);
    if (isqk) {
      const bool isq = ct < 5;
      const float sc = isq ? QSCALE : 1.f;
      const unsigned ts = isq ? 1536 : 3072;
      char* base = ws + (isq ? WS_Q : WS_KV) + (size_t)tile0 * ts;
#pragma unroll
      for (int n = 0; n < 2; ++n) {
        const int ub = (isq ? ct : ct - 5) * 16 + wc * 8 + n * 4;
#pragma unroll
        for (int gp = 0; gp < 2; ++gp) {
          const int U0 = ub + 2 * gp;
          const unsigned o0 = unit_off(U0, 64 * ts), o1 = unit_off(U0 + 1, 64 * ts);
          const unsigned voff = r * 16 + h * (o1 - o0);
          const bool mixed = ((U0 + h) % 5) == 2;
#pragma unroll
          for (int m = 0; m < 2; ++m) {
            const f32x16& a = acc[m][n];
            unsigned a0 = pk2(a[8 * gp] * sc, a[8 * gp + 1] * sc), a1 = pk2(a[8 * gp + 2] * sc, a[8 * gp + 3] * sc);
            unsigned b0 = pk2(a[8 * gp + 4] * sc, a[8 * gp + 5] * sc), b1 = pk2(a[8 * gp + 6] * sc, a[8 * gp + 7] * sc);
            auto s0 = __builtin_amdgcn_permlane32_swap(a0, b0, false, false);
            auto s1 = __builtin_amdgcn_permlane32_swap(a1, b1, false, false);
            u32x4 v = {s0[0], s1[0], s0[1], s1[1]};
            char* p = base + o0 + m * ts + voff;
            st16(p, v);
            if (mixed) st16(p + 64 * ts, v);
          }
        }
      }
    } else {
#pragma unroll
      for (int n = 0; n < 2; ++n) {
        const int c32 = (ct - 15) * 4 + wc * 2 + n;
        if (c32 < 54) {
#pragma unroll
          for (int m = 0; m < 2; ++m) {
#pragma unroll
            for (int s = 0; s < 2; ++s) {
              u32x4 v;
#pragma unroll
              for (int q = 0; q < 4; ++q) v[q] = relu_pk(pk2(acc[m][n][8 * s + 2 * q], acc[m][n][8 * s + 2 * q + 1]));
              st16(ws + WS_H + ((size_t)((tile0 + m) * 2 + (r >> 4)) * 54 + c32) * 1024 + ((2 * s + h) * 16 + (r & 15)) * 16, v);
            }
          }
        }
      }
    }
  }
  DBGDUMP(K1_NS * 49152, 1);
}

#define FFN_PRIO 0
constexpr int K2_LDS = 81920;
#define POLL_SLEEP 4

DI float xhalf_max(float m) {
  auto rr = __builtin_amdgcn_permlane32_swap(__float_as_uint(m), __float_as_uint(m), false, false);
  return fmaxf(__uint_as_float(rr[0]), __uint_as_float(rr[1]));
}
DI float max16(const f32x16& s) {
  float a = fmaxf(fmaxf(s[0], s[1]), s[2]), b = fmaxf(fmaxf(s[3], s[4]), s[5]);
  a = fmaxf(fmaxf(a, s[6]), s[7]); b = fmaxf(fmaxf(b, s[8]), s[9]);
  a = fmaxf(fmaxf(a, s[10]), s[11]); b = fmaxf(fmaxf(b, s[12]), s[13]);
  a = fmaxf(fmaxf(a, s[14]), s[15]);
  return fmaxf(a, b);
}
DI void wait_count(gu32* flag, unsigned target) {
  for (unsigned spins = 0; spins < (1u << 18); ++spins) {
    if ((unsigned)__builtin_amdgcn_readfirstlane(__hip_atomic_load(flag, __ATOMIC_RELAXED, __HIP_MEMORY_SCOPE_AGENT)) >= target) break;
    __builtin_amdgcn_s_sleep(2);
  }
}

DI void attn_wg(char* __restrict__ ws, lds_char* lds, int bid) {
  const int tid = threadIdx.x, l = tid & 63;
  const int w = __builtin_amdgcn_readfirstlane(tid >> 6);
  const int r = l & 31, h = l >> 5;
  const int xcd = bid & 7, idx = bid >> 3;
  const int head = xcd * 4 + (idx >> 3), qb = idx & 7;
  const int qt = qb * 8 + w;
  const char* kv = ws + WS_KV + (size_t)head * 64 * 3072;
  const unsigned lds0 = (unsigned)(uintptr_t)lds;
  const char* kvp = kv + (w < 6 ? w : 0) * 1024;
  const unsigned kvdst = lds0 + (w < 6 ? w * 1024 : 24576), kvstep = w < 6 ? 6144 : 0;
  auto dma_kv = [&](int ch, int U) { glds16(kvp + (size_t)ch * 6144, l * 16, kvdst + U * kvstep); };
  TL(0);
  const char* qp = ws + WS_Q + (size_t)(head * 64 + qt) * 1536;
  bf16x8 q1;
  u32x2 q2u;
  asm volatile("global_load_dwordx4 %0, %1, %2" : "=v"(q1) : "v"(l * 16), "s"(qp) : "memory");
  asm volatile("global_load_dwordx2 %0, %1, %2" : "=v"(q2u) : "v"(1024 + r * 16 + (head & 1) * 8), "s"(qp) : "memory");
  dma_kv(0, 0); dma_kv(1, 1); dma_kv(2, 2);
  const unsigned k1rd = l * 16, k2rd = h ? 1536 + 20 * 16 : 1024 + r * 16 + (head & 1) * 8;
  const int dd = r < 24 ? r : (r < 28 ? r - 4 : r - 12);
  const unsigned vrd = 1536 + h * 384 + dd * 16;
  f32x16 zero;
#pragma unroll
  for (int i = 0; i < 16; ++i) zero[i] = 0.f;
  asm volatile("s_waitcnt vmcnt(2)" : "+v"(q1), "+v"(q2u)::"memory");
  RAW_BAR();
  if (h) { q2u[0] = 0; q2u[1] = 0; }
  bf16x8 kf1 = LDS128(lds + k1rd);
  bf16x4 kf2 = LDS64(lds + k2rd);
  {
    f32x16 s = MFMA16(kf1, q1, zero);
    s = MFMA8(kf2, __builtin_bit_cast(bf16x4, q2u), s);
    float m = xhalf_max(max16(s));
    if (h) q2u[0] = pk2(-m, 0.f);
  }
  const bf16x4 q2 = __builtin_bit_cast(bf16x4, q2u);
  f32x16 o = zero;
  f32x16 sA = MFMA16(kf1, q1, zero);
  sA = MFMA8(kf2, q2, sA);
  f32x16 sB;
  kf1 = LDS128(lds + 3072 + k1rd);
  kf2 = LDS64(lds + 3072 + k2rd);
  bf16x8 vf0 = LDS128(lds + vrd);
  bf16x8 vf1 = vf0;
  u32x4 p0, p1 = {0u, 0u, 0u, 0u};
#define AT_STEP(SC, SN, KN, VC, VN, D0) do { \
    o = MFMA16(vf1, __builtin_bit_cast(bf16x8, p1), o); PIN(o); \
    vf1 = LDS128((VC) + vrd + 768); SBAR(); \
    SC[0] = EX(SC[0]); SC[1] = EX(SC[1]); SC[2] = EX(SC[2]); SC[3] = EX(SC[3]); PIN(SC); SBAR(); \
    SN = MFMA16(kf1, q1, zero); \
    kf1 = LDS128((KN) + k1rd); SBAR(); \
    D0; \
    SC[4] = EX(SC[4]); SC[5] = EX(SC[5]); SC[6] = EX(SC[6]); SC[7] = EX(SC[7]); \
    p0[0] = pk2(SC[0], SC[1]); p0[1] = pk2(SC[2], SC[3]); p0[2] = pk2(SC[4], SC[5]); p0[3] = pk2(SC[6], SC[7]); PIN(p0); SBAR(); \
    o = MFMA16(vf0, __builtin_bit_cast(bf16x8, p0), o); PIN(o); \
    vf0 = LDS128((VN) + vrd); SBAR(); \
    SC[8] = EX(SC[8]); SC[9] = EX(SC[9]); SC[10] = EX(SC[10]); SC[11] = EX(SC[11]); PIN(SC); SBAR(); \
    SN = MFMA8(kf2, q2, SN); \
    kf2 = LDS64((KN) + k2rd); SBAR(); \
    SC[12] = EX(SC[12]); SC[13] = EX(SC[13]); SC[14] = EX(SC[14]); SC[15] = EX(SC[15]); \
    p1[0] = pk2(SC[8], SC[9]); p1[1] = pk2(SC[10], SC[11]); p1[2] = pk2(SC[12], SC[13]); p1[3] = pk2(SC[14], SC[15]); PIN(p1); SBAR(); \
  } while (0)
#define NOP_ ((void)0)
#define SUPER(U, WN, X0) do { \
    WAIT_VM(WN); \
    RAW_BAR(); \
    AT_STEP(sA, sB, lds + (((U) + 1) & 3) * 6144, lds + (U) * 6144, lds + (U) * 6144 + 3072, X0); \
    AT_STEP(sB, sA, lds + (((U) + 1) & 3) * 6144 + 3072, lds + (U) * 6144 + 3072, lds + (((U) + 1) & 3) * 6144, NOP_); \
  } while (0)
  TL(1);
#pragma unroll 1
  for (int c = 0; c < 28; c += 4) {
    SUPER(0, 1, dma_kv(c + 3, 3)); SUPER(1, 1, dma_kv(c + 4, 0)); SUPER(2, 1, dma_kv(c + 5, 1)); SUPER(3, 1, dma_kv(c + 6, 2));
  }
  SUPER(0, 1, dma_kv(31, 3)); SUPER(1, 1, NOP_); SUPER(2, 0, NOP_);
  if (tid == 0) __hip_atomic_fetch_add((gu32*)(ws + WS_FLAG + qb * 256), 1u, __ATOMIC_RELAXED, __HIP_MEMORY_SCOPE_AGENT);
  SUPER(3, 63, NOP_);
  o = MFMA16(vf1, __builtin_bit_cast(bf16x8, p1), o);
  TL(2);
  float lsum;
  {
    auto rr = __builtin_amdgcn_permlane32_swap(__float_as_uint(o[8]), __float_as_uint(o[8]), false, false);
    lsum = __uint_as_float(rr[1]);
  }
  bool okv = lsum < 1.0e18f;
#pragma unroll
  for (int i = 0; i < 12; ++i) okv = okv && ((h && i >= 8) ? true : fabsf(o[i]) < 1.0e30f);
  if (!__all(okv)) {
    u32x2 qz = q2u; if (h) qz[0] = 0;
    const bf16x4 q2z = __builtin_bit_cast(bf16x4, qz);
    float m = -INFINITY;
    o = zero;
    for (int t = 0; t < 64; ++t) {
      const char* tb = kv + (size_t)t * 3072;
      bf16x8 k1 = *(const bf16x8*)(tb + k1rd);
      bf16x4 k2 = *(const bf16x4*)(tb + k2rd);
      bf16x8 v0 = *(const bf16x8*)(tb + vrd);
      bf16x8 v1 = *(const bf16x8*)(tb + vrd + 768);
      f32x16 s = MFMA16(k1, q1, zero);
      s = MFMA8(k2, q2z, s);
      float mn = fmaxf(m, xhalf_max(max16(s)));
      float alpha = EX(m - mn);
      m = mn;
#pragma unroll
      for (int i = 0; i < 16; ++i) o[i] *= alpha;
      u32x4 a0, a1;
#pragma unroll
      for (int j = 0; j < 4; ++j) {
        a0[j] = pk2(EX(s[2 * j] - m), EX(s[2 * j + 1] - m));
        a1[j] = pk2(EX(s[8 + 2 * j] - m), EX(s[8 + 2 * j + 1] - m));
      }
      o = MFMA16(v0, __builtin_bit_cast(bf16x8, a0), o);
      o = MFMA16(v1, __builtin_bit_cast(bf16x8, a1), o);
    }
    auto rr = __builtin_amdgcn_permlane32_swap(__float_as_uint(o[8]), __float_as_uint(o[8]), false, false);
    lsum = __uint_as_float(rr[1]);
  }
  const float inv = 1.f / lsum;
  char* ob = ws + WS_O + (size_t)(qt * 2 + (r >> 4)) * 20 * 1024 + (r & 15) * 16;
#pragma unroll
  for (int g = 0; g < 3; ++g) {
    const int gi = 2 * g + h;
    if (gi < 5) {
      const int G = 5 * head + gi;
      u32x2 v;
      v[0] = pk2(o[4 * g] * inv, o[4 * g + 1] * inv);
      v[1] = pk2(o[4 * g + 2] * inv, o[4 * g + 3] * inv);
      __hip_atomic_store((gu64*)(ob + (G >> 3) * 1024 + (G & 3) * 256 + ((G >> 2) & 1) * 8), __builtin_bit_cast(unsigned long long, v),
                         __ATOMIC_RELAXED, __HIP_MEMORY_SCOPE_AGENT);
    }
  }
  TL(3);
}

DI void out_wg(const u16* __restrict__ w2, const u16* __restrict__ wo, char* __restrict__ ws, u16* __restrict__ out, lds_char* lds, int j) {
  const int tid = threadIdx.x, l = tid & 63;
  const int w = __builtin_amdgcn_readfirstlane(tid >> 6);
  const unsigned lds0 = (unsigned)(uintptr_t)lds;
  const int wt = w & 3, kh = w >> 2;
  const int rt = j & 31, ct = j >> 5;
  const int tk = l & 15, kq = l >> 4;
  const int token = rt * 64 + wt * 16 + tk;
  lds_char* red = lds + wt * 5120 + l * 16;
  const unsigned roff = (l / 10) * 1280 + (l % 10) * 16;
  constexpr unsigned long long M60 = (1ull << 60) - 1, M40 = (1ull << 40) - 1;
  TL(0);
  float bt[3][4];
  const float g = ((const float*)(ws + WS_G))[token];
  {
    const char* wb2 = (const char*)w2 + ct * 160;
    auto wstage = [&](int st, int slot) {
#pragma unroll
      for (int i = 0; i < 4; ++i) glds16m(wb2 + (size_t)(st * 192 + (i * 8 + w) * 6) * 1280, roff, lds0 + slot * 30720 + (i * 8 + w) * 960, M60);
    };
    const bf16x8* hp = (const bf16x8*)(ws + WS_H + (size_t)((rt * 4 + wt) * 54 + kh * 3) * 1024) + l;
    bf16x8 ha[3], hb[3];
#pragma unroll
    for (int i = 0; i < 3; ++i) ha[i] = hp[i * 64];
    wstage(0, 0);
    f32x4 accb[5];
#pragma unroll
    for (int n = 0; n < 5; ++n) accb[n] = f32x4{0.f, 0.f, 0.f, 0.f};
    const unsigned br2 = (kh * 96 + 16 * (kq >> 1) + 4 * (kq & 1) + (tk >> 2)) * 160 + (tk & 3) * 8;
    auto fstage = [&](int st, int slot, bf16x8 (&hc)[3], bf16x8 (&hn)[3], bool more) {
      WAIT_VM(0);
      RAW_BAR();
      if (more) {
#pragma unroll
        for (int i = 0; i < 3; ++i) hn[i] = hp[((st + 1) * 6 + i) * 64];
        wstage(st + 1, slot ^ 1);
      }
      lds_char* sb = lds + slot * 30720 + br2;
#pragma unroll
      for (int i = 0; i < 3; ++i)
#pragma unroll
        for (int n = 0; n < 5; ++n) {
          bf16x4 lo = tr_read(sb + i * 32 * 160 + n * 32);
          bf16x4 hi = tr_read(sb + (i * 32 + 8) * 160 + n * 32);
          accb[n] = MFMAS(__builtin_shufflevector(lo, hi, 0, 1, 2, 3, 4, 5, 6, 7), hc[i], accb[n]);
        }
    };
#pragma unroll 1
    for (int st = 0; st < 8; st += 2) { fstage(st, 0, ha, hb, true); fstage(st + 1, 1, hb, ha, true); }
    fstage(8, 0, ha, hb, false);
    __builtin_amdgcn_s_setprio(1);
    RAW_BAR();
#pragma unroll
    for (int n = 0; n < 5; ++n)
      if ((n < 3) != (kh == 0)) *(__attribute__((address_space(3))) f32x4*)(red + n * 1024) = accb[n];
    RAW_BAR();
    const float omg = rbf(1.f - g);
#pragma unroll
    for (int n = 0; n < 5; ++n)
      if ((n < 3) == (kh == 0)) {
        f32x4 a2 = *(__attribute__((address_space(3))) f32x4*)(red + n * 1024);
#pragma unroll
        for (int i = 0; i < 4; ++i) bt[n % 3][i] = rbf(omg * rbf(accb[n][i] + a2[i]));
      }
    RAW_BAR();
  }
  TL(1);
  const char* wb = (const char*)wo + ct * 160;
#pragma unroll
  for (int i = 0; i < 3; ++i) {
    const int pc = i * 8 + w, blk = pc >= 11, pp = pc - blk * 11;
    if (pc < 22) glds16m(wb + (size_t)(blk * 320 + pp * 6) * 1280, roff, lds0 + (blk * 64 + pp * 6) * 160, pp == 10 ? M40 : M60);
  }
  WAIT_VM(0);
  RAW_BAR();
  const unsigned brl = (4 * kq + (tk >> 2)) * 160 + (tk & 3) * 8;
  bf16x8 wf[2][5];
#pragma unroll
  for (int ks = 0; ks < 2; ++ks)
#pragma unroll
    for (int n = 0; n < 5; ++n) {
      bf16x4 lo = tr_read(lds + brl + (kh * 64 + ks * 32) * 160 + n * 32);
      bf16x4 hi = tr_read(lds + brl + (kh * 64 + ks * 32 + 16) * 160 + n * 32);
      wf[ks][n] = __builtin_shufflevector(lo, hi, 0, 1, 2, 3, 4, 5, 6, 7);
    }
  RAW_BAR();
#pragma unroll
  for (int i = 0; i < 11; ++i) {
    const int pc = i * 8 + w, blk = pc >= 43, pp = pc - blk * 43;
    if (pc < 86) glds16m(wb + (size_t)(64 + blk * 320 + pp * 6) * 1280, roff, lds0 + (blk * 256 + pp * 6) * 160, pp == 42 ? M40 : M60);
  }
  TL(2);
  if (w == 0) wait_count((gu32*)(ws + WS_FLAG + (rt >> 2) * 256), 32u);
  WAIT_VM(0);
  RAW_BAR();
  __builtin_amdgcn_s_setprio(0);
  TL(3);
  const char* op = ws + WS_O + (size_t)((rt * 4 + wt) * 20 + kh * 10) * 1024;
  u32x4 of[10];
  for (unsigned spins = 0; spins < (1u << 16); ++spins) {
    asm volatile("global_load_dwordx4 %0, %10, %11 sc1\n\tglobal_load_dwordx4 %1, %10, %11 offset:1024 sc1\n\t"
                 "global_load_dwordx4 %2, %10, %11 offset:2048 sc1\n\tglobal_load_dwordx4 %3, %10, %11 offset:3072 sc1\n\t"
                 "global_load_dwordx4 %4, %12, %11 sc1\n\tglobal_load_dwordx4 %5, %12, %11 offset:1024 sc1\n\t"
                 "global_load_dwordx4 %6, %12, %11 offset:2048 sc1\n\tglobal_load_dwordx4 %7, %12, %11 offset:3072 sc1\n\t"
                 "global_load_dwordx4 %8, %13, %11 sc1\n\tglobal_load_dwordx4 %9, %13, %11 offset:1024 sc1\n\ts_waitcnt vmcnt(0)"
                 : "=&v"(of[0]), "=&v"(of[1]), "=&v"(of[2]), "=&v"(of[3]), "=&v"(of[4]), "=&v"(of[5]), "=&v"(of[6]), "=&v"(of[7]), "=&v"(of[8]), "=&v"(of[9])
                 : "v"(l * 16), "s"(op), "v"(l * 16 + 4096), "v"(l * 16 + 8192) : "memory");
    unsigned mx = 0;
#pragma unroll
    for (int ks = 0; ks < 10; ++ks) mx = max(max(mx, max(of[ks][0], of[ks][1])), max(of[ks][2], of[ks][3]));
    if (__all(mx != 0xFFFFFFFFu)) break;
    __builtin_amdgcn_s_sleep(POLL_SLEEP);
  }
  __builtin_amdgcn_s_setprio(3);
  f32x4 acc[5];
#pragma unroll
  for (int n = 0; n < 5; ++n) acc[n] = f32x4{0.f, 0.f, 0.f, 0.f};
  TL(4);
#pragma unroll
  for (int ks = 0; ks < 10; ++ks) {
#pragma unroll
    for (int n = 0; n < 5; ++n) acc[n] = MFMAS(wf[ks & 1][n], __builtin_bit_cast(bf16x8, of[ks]), acc[n]);
    if (ks + 2 < 10) {
#pragma unroll
      for (int n = 0; n < 5; ++n) {
        bf16x4 lo = tr_read(lds + brl + (kh * 256 + ks * 32) * 160 + n * 32);
        bf16x4 hi = tr_read(lds + brl + (kh * 256 + ks * 32 + 16) * 160 + n * 32);
        wf[ks & 1][n] = __builtin_shufflevector(lo, hi, 0, 1, 2, 3, 4, 5, 6, 7);
      }
    }
    SBAR();
  }
  RAW_BAR();
  TL(5);
#pragma unroll
  for (int n = 0; n < 5; ++n)
    if ((n < 3) != (kh == 0)) *(__attribute__((address_space(3))) f32x4*)(red + n * 1024) = acc[n];
  RAW_BAR();
  const size_t eo = (size_t)token * DM + ct * 80 + 4 * kq;
#pragma unroll
  for (int n = 0; n < 5; ++n) {
    if ((n < 3) == (kh == 0)) {
      f32x4 a2 = *(__attribute__((address_space(3))) f32x4*)(red + n * 1024);
      float v[4];
#pragma unroll
      for (int i = 0; i < 4; ++i) v[i] = rbf(g * rbf(acc[n][i] + a2[i])) + bt[n % 3][i];
      u32x2 o2;
      o2[0] = pk2(v[0], v[1]);
      o2[1] = pk2(v[2], v[3]);
      *(u32x2*)(out + eo + n * 16) = o2;
    }
  }
  TL(6);
}

__global__ __launch_bounds__(512, 4) void k2_kernel(const u16* __restrict__ w2, const u16* __restrict__ wo, char* __restrict__ ws,
                                                    u16* __restrict__ out) {
  extern __shared__ __attribute__((aligned(16))) char smem[];
  lds_char* lds = (lds_char*)smem;
  const int bid = blockIdx.x;
  if (bid < 256) attn_wg(ws, lds, bid);
  else { __builtin_amdgcn_s_setprio(FFN_PRIO); out_wg(w2, wo, ws, out, lds, bid - 256); }
}

extern "C" void kernel_launch(void* const* d_in, const int* in_sizes, int n_in, void* d_out, int out_size, void* d_ws, size_t ws_size,
                              hipStream_t stream) {
  const u16* x = (const u16*)d_in[0];
  const u16* wqkv = (const u16*)d_in[1];
  const u16* wo = (const u16*)d_in[2];
  const u16* w1 = (const u16*)d_in[4];
  const u16* w2 = (const u16*)d_in[5];
  const u16* gw = (const u16*)d_in[6];
  char* ws = (char*)d_ws;
  hipFuncSetAttribute((const void*)k1_kernel, hipFuncAttributeMaxDynamicSharedMemorySize, K1_LDS);
  hipFuncSetAttribute((const void*)k2_kernel, hipFuncAttributeMaxDynamicSharedMemorySize, K2_LDS);
  k1_kernel<<<256, 512, K1_LDS, stream>>>(x, wqkv, w1, gw, ws);
  k2_kernel<<<512, 512, K2_LDS, stream>>>(w2, wo, ws, (u16*)d_out);
}
```

```cpp
#include <hip/hip_runtime.h>
#include <hip/hip_bf16.h>
#include <stdint.h>
#include <type_traits>

typedef short bf16x8 __attribute__((ext_vector_type(8)));
typedef short bf16x4 __attribute__((ext_vector_type(4)));
typedef float f32x16 __attribute__((ext_vector_type(16)));
typedef float f32x4 __attribute__((ext_vector_type(4)));
typedef float f32x2 __attribute__((ext_vector_type(2)));
typedef __bf16 bfx2 __attribute__((ext_vector_type(2)));
typedef unsigned u32x4 __attribute__((ext_vector_type(4)));
typedef unsigned u32x2 __attribute__((ext_vector_type(2)));
typedef __attribute__((address_space(3))) char lds_char;
typedef __attribute__((address_space(1))) unsigned gu32;
typedef __attribute__((address_space(1))) unsigned long long gu64;
typedef unsigned short u16;

#define DI __device__ __forceinline__
#define MFMA16(a, b, c) __builtin_amdgcn_mfma_f32_32x32x16_bf16((a), (b), (c), 0, 0, 0)
#define MFMA8(a, b, c) __builtin_amdgcn_mfma_f32_32x32x8bf16_1k((a), (b), (c), 0, 0, 0)
#define MFMAS(a, b, c) __builtin_amdgcn_mfma_f32_16x16x32_bf16((a), (b), (c), 0, 0, 0)

constexpr int SEQ = 2048, DM = 640, NH = 32, DH = 20, DFF = 1728, NQKV = 1920;

constexpr size_t WS_KV = 0;
constexpr size_t WS_Q = WS_KV + (size_t)32 * 64 * 3072;
constexpr size_t WS_H = WS_Q + (size_t)32 * 64 * 1536;
constexpr size_t WS_O = WS_H + (size_t)64 * 108 * 1024;
constexpr size_t WS_G = WS_O + (size_t)128 * 20 * 1024;
constexpr size_t WS_FLAG = WS_G + 2048 * 4;

DI unsigned pk2(float lo, float hi) {
  f32x2 v = {lo, hi};
  bfx2 r = __builtin_convertvector(v, bfx2);
  return __builtin_bit_cast(unsigned, r);
}
DI void st16(void* p, u32x4 v) { __builtin_nontemporal_store(v, (u32x4*)p); }
DI unsigned relu_pk(unsigned v) {
  typedef short s16x2 __attribute__((ext_vector_type(2)));
  s16x2 a = __builtin_bit_cast(s16x2, v), z = {0, 0};
  return __builtin_bit_cast(unsigned, __builtin_elementwise_max(a, z));
}
DI float rbf(float x) { return (float)(__bf16)x; }
DI float bfbits2f(unsigned short b) { return __uint_as_float((unsigned)b << 16); }

DI void glds16(const char* sbase, unsigned voff, unsigned lds_dst) {
  unsigned keep;
  lds_dst = __builtin_amdgcn_readfirstlane(lds_dst);
  asm volatile("s_mov_b32 %0, m0\n\ts_mov_b32 m0, %3\n\ts_nop 0\n\tglobal_load_lds_dwordx4 %1, %2\n\ts_mov_b32 m0, %0"
               : "=&s"(keep) : "v"(voff), "s"(sbase), "s"(lds_dst) : "memory");
}
DI void glds16m(const char* sbase, unsigned voff, unsigned lds_dst, unsigned long long mask) {
  unsigned keep;
  lds_dst = __builtin_amdgcn_readfirstlane(lds_dst);
  asm volatile("s_mov_b32 %0, m0\n\ts_mov_b32 m0, %3\n\ts_mov_b64 exec, %4\n\tglobal_load_lds_dwordx4 %1, %2\n\ts_mov_b64 exec, -1\n\ts_mov_b32 m0, %0"
               : "=&s"(keep) : "v"(voff), "s"(sbase), "s"(lds_dst), "s"(mask) : "memory");
}
typedef short v4i16_t __attribute__((ext_vector_type(4)));
DI bf16x4 tr_read(lds_char* p) {
  return __builtin_bit_cast(bf16x4, __builtin_amdgcn_ds_read_tr16_b64_v4i16((__attribute__((address_space(3))) v4i16_t*)p));
}
#define SBAR() __builtin_amdgcn_sched_barrier(0)
#define PIN(x) asm volatile("" : "+v"(x))
#define WAIT_VM(n) asm volatile("s_waitcnt vmcnt(%0)" ::"n"(n) : "memory")
#define RAW_BAR() do { asm volatile("" ::: "memory"); __builtin_amdgcn_s_waitcnt(0xC07F); __builtin_amdgcn_s_barrier(); asm volatile("" ::: "memory"); } while (0)
#define LDS128(p) (*(__attribute__((address_space(3))) bf16x8*)(p))
#define LDS64(p) (*(__attribute__((address_space(3))) bf16x4*)(p))
#define EX(x) __builtin_amdgcn_exp2f(x)


template <int MT, int NT> struct Frag { bf16x8 a[MT]; bf16x4 bl[NT], bh[NT]; };

template <int MT, int NT, int AMODE, bool SWAP, int NS>
DI void gemm8(f32x16 (&acc)[MT][NT], const char* Abase, unsigned a_rt_stride, const char* Bbase, unsigned ldb_bytes, int cmax, int nk,
              lds_char* lds) {
  constexpr int BM = 128 * MT, BN = 64 * NT, BK = 64, KS = 4;
  constexpr int ABYTES = BM * BK * 2, BBYTES = BK * BN * 2, STAGE = ABYTES + BBYTES;
  constexpr int APW = ABYTES / 8192, BPW = BBYTES / 8192, NLD = APW + BPW;
  const int tid = threadIdx.x, l = tid & 63;
  const int w = __builtin_amdgcn_readfirstlane(tid >> 6);
  const int wr = w >> 1, wc = w & 1;
  const int r = l & 31, h = l >> 5;
  unsigned aoff[APW];
#pragma unroll
  for (int i = 0; i < APW; ++i) {
    const int pid = APW * w + i;
    if (AMODE == 0) {
      int row = 8 * pid + (l >> 3);
      aoff[i] = row * 1280 + (((l & 7) ^ ((row >> 1) & 7)) * 16);
    } else {
      aoff[i] = (pid / KS) * a_rt_stride + (pid % KS) * 1024 + l * 16;
    }
  }
  constexpr int BROWS = (BN == 128) ? 4 : 8;
  unsigned boff;
  if (BN == 128) {
    int c = (l & 15) ^ ((l >> 4) << 2);
    c = c < cmax ? c : cmax;
    boff = (BROWS * BPW * w + (l >> 4)) * ldb_bytes + c * 16;
  } else {
    int c = (l & 7) ^ (((l >> 4) & 1) << 2);
    c = c < cmax ? c : cmax;
    boff = (BROWS * BPW * w + (l >> 3)) * ldb_bytes + c * 16;
  }
  const unsigned lds0 = (unsigned)(uintptr_t)lds;
  auto piece = [&](int kt, int slot, int i) {
    const unsigned dst = lds0 + slot * STAGE;
    if (i < APW) {
      const char* ap = Abase + (AMODE == 0 ? kt * (BK * 2) : kt * (KS * 1024));
      glds16(ap, aoff[i < APW ? i : 0], dst + (APW * w + i) * 1024);
    } else {
      const char* bp = Bbase + (size_t)kt * BK * ldb_bytes;
      glds16(bp + (i - APW) * BROWS * ldb_bytes, boff, dst + ABYTES + (BPW * w + (i - APW)) * 1024);
    }
  };
  auto issue = [&](int kt, int slot) {
#pragma unroll
    for (int i = 0; i < NLD; ++i) piece(kt, slot, i);
  };
  unsigned ard[KS];
#pragma unroll
  for (int ks = 0; ks < KS; ++ks) {
    if (AMODE == 0) ard[ks] = (wr * (32 * MT) + r) * 128 + (((2 * ks + h) ^ ((r >> 1) & 7)) * 16);
    else ard[ks] = (wr * MT) * 4096 + ks * 1024 + l * 16;
  }
  unsigned brd[NT];
  {
    const int q = (l & 15) >> 2, p = l & 3, cbh = (l >> 4) & 1;
    const int krow = (AMODE == 0 ? 8 * h : 4 * h) + q;
#pragma unroll
    for (int n = 0; n < NT; ++n) {
      int chunk = (wc * NT + n) * 4 + 2 * cbh + (p >> 1);
      if (BN == 128) chunk ^= (q << 2); else chunk ^= (((q >> 1) & 1) << 2);
      brd[n] = ABYTES + krow * (BN * 2) + chunk * 16 + (p & 1) * 8;
    }
  }
  constexpr int TOFF = (AMODE == 0 ? 4 : 8) * (BN * 2);
  auto ldA = [&](Frag<MT, NT>& f, lds_char* sb, int ks, int m) { f.a[m] = LDS128(sb + ard[ks] + m * 4096); };
  auto ldB = [&](Frag<MT, NT>& f, lds_char* sb, int ks, int n) {
    f.bl[n] = tr_read(sb + brd[n] + ks * 16 * (BN * 2));
    f.bh[n] = tr_read(sb + brd[n] + ks * 16 * (BN * 2) + TOFF);
  };
  auto mma = [&](Frag<MT, NT>& f, int m, int n) {
    bf16x8 bb = __builtin_shufflevector(f.bl[n], f.bh[n], 0, 1, 2, 3, 4, 5, 6, 7);
    acc[m][n] = SWAP ? MFMA16(bb, f.a[m], acc[m][n]) : MFMA16(f.a[m], bb, acc[m][n]);
  };
  auto step = [&](Frag<MT, NT>& cur, Frag<MT, NT>& nxt, lds_char* nsb, int nks, bool pf, int pkt, int pslot, int P0, int P1) {
    mma(cur, 0, 0);
    ldB(nxt, nsb, nks, 0); ldA(nxt, nsb, nks, 0);
    SBAR();
    if (NT == 2) { mma(cur, 0, NT - 1); ldB(nxt, nsb, nks, NT - 1); }
    if (MT == 2) ldA(nxt, nsb, nks, MT - 1);
    SBAR();
    if (pf && P0 < P1) piece(pkt, pslot, P0);
    if (MT == 2) { mma(cur, MT - 1, 0); SBAR(); }
    if (pf) { for (int i = P0 + 1; i < P1; ++i) piece(pkt, pslot, i); }
    if (MT == 2 && NT == 2) mma(cur, MT - 1, NT - 1);
    SBAR();
  };
  constexpr int PQ = (NLD + 2) / 3;
  constexpr int P1 = PQ, P2 = 2 * PQ < NLD ? 2 * PQ : NLD;
  issue(0, 0);
  issue(1, 1);
  issue(2, 2);
  Frag<MT, NT> X, Y;
  WAIT_VM(2 * NLD);
  RAW_BAR();
#pragma unroll
  for (int n = 0; n < NT; ++n) ldB(X, lds, 0, n);
#pragma unroll
  for (int m = 0; m < MT; ++m) ldA(X, lds, 0, m);
  int slot = 0, pslot = NS - 1;
  static_assert(NS == 3, "");
  auto body = [&](int t, auto pf_c, auto fl_c) {
    constexpr bool pf = decltype(pf_c)::value, fl = decltype(fl_c)::value;
    lds_char* sb = lds + slot * STAGE;
    const int pkt = t + NS - 1;
    step(X, Y, sb, 1, pf, pkt, pslot, 0, P1);
    step(Y, X, sb, 2, pf, pkt, pslot, P1, P2);
    step(X, Y, sb, 3, pf, pkt, pslot, P2, NLD);
    if (fl) WAIT_VM(NLD); else WAIT_VM(0);
    RAW_BAR();
    pslot = slot;
    slot = (slot + 1 == NS) ? 0 : slot + 1;
    step(Y, X, lds + slot * STAGE, 0, false, 0, 0, 0, 0);
  };
  body(0, std::false_type{}, std::true_type{});
#pragma unroll 1
  for (int t = 1; t < nk - 2; ++t) body(t, std::true_type{}, std::true_type{});
#pragma unroll 1
  for (int t = 0; t < 2; ++t) body(t, std::false_type{}, std::false_type{});
  RAW_BAR();
}

constexpr int K1_NS = 3;
constexpr int K1_LDS = K1_NS * 49152;
constexpr float QSCALE = 0.22360679774997896f * 1.4426950408889634f;

DI unsigned unit_off(int U, unsigned head_stride) {
  const int p = U / 5, ui = U - p * 5;
  return (2 * p + (ui >= 3 ? 1 : 0)) * head_stride + (ui == 2 ? 1024 : (ui >= 3 ? ui - 3 : ui) * 512);
}

__global__ __launch_bounds__(512, 2) void k1_kernel(const u16* __restrict__ x, const u16* __restrict__ wqkv, const u16* __restrict__ w1,
                                                    const u16* __restrict__ gw, char* __restrict__ ws) {
  extern __shared__ __attribute__((aligned(16))) char smem[];
  lds_char* lds = (lds_char*)smem;
  const int bid = blockIdx.x, tid = threadIdx.x, l = tid & 63;
  const int w = __builtin_amdgcn_readfirstlane(tid >> 6);
  if (bid >= 232) {
    if (bid < 248) {
#pragma unroll
      for (int half = 0; half < 2; ++half) {
        const int row = (bid - 232) * 128 + half * 64 + w * 8 + (l >> 3), sub = l & 7;
        float s = 0.f;
#pragma unroll
        for (int i = 0; i < 10; ++i) {
          const int c = (i * 8 + sub) * 8;
          bf16x8 xv = *(const bf16x8*)(x + (size_t)row * DM + c);
          bf16x8 gv = *(const bf16x8*)(gw + c);
#pragma unroll
          for (int q = 0; q < 8; ++q) s += rbf(bfbits2f((u16)xv[q]) * bfbits2f((u16)gv[q]));
        }
        s += __shfl_xor(s, 1); s += __shfl_xor(s, 2); s += __shfl_xor(s, 4);
        s = rbf(s);
        float g = rbf(1.f / (1.f + __expf(-s)));
        if (sub == 0) ((float*)(ws + WS_G))[row] = g;
      }
    } else {
      const u32x4 sn = {0xFFFFFFFFu, 0xFFFFFFFFu, 0xFFFFFFFFu, 0xFFFFFFFFu};
      char* p = ws + WS_O + (size_t)(bid - 248) * 327680 + tid * 16;
#pragma unroll 8
      for (int i = 0; i < 40; ++i) st16(p + i * 8192, sn);
    }
    return;
  }
  const int lin = ((bid & 7) >> 1) * 29 + (bid >> 3);
  const int rowp = (bid & 1) * 4 + (lin & 3), ct = lin >> 2;
  const int wr = w >> 1, wc = w & 1, r = l & 31, h = l >> 5;
  if (bid == 0 && tid < 24) *(unsigned*)(ws + WS_FLAG + tid * 256) = 0u;
  const char* Abase = (const char*)x + (size_t)rowp * 256 * 1280;
  const int tile0 = rowp * 8 + wr * 2;
  f32x16 acc[2][2];
#pragma unroll
  for (int m = 0; m < 2; ++m)
#pragma unroll
    for (int n = 0; n < 2; ++n)
#pragma unroll
      for (int i = 0; i < 16; ++i) acc[m][n][i] = 0.f;
  if (ct >= 10 && ct < 15) {
    gemm8<2, 2, 0, false, K1_NS>(acc, Abase, 0, (const char*)wqkv + ct * 256, NQKV * 2, 15, 10, lds);
#pragma unroll
    for (int n = 0; n < 2; ++n) {
      const int f = (ct - 10) * 128 + wc * 64 + n * 32 + r;
      const int head = f / 20, d = f - head * 20;
      char* base = ws + WS_KV + (size_t)(head * 64 + tile0) * 3072 + 1536 + h * 384 + d * 16;
#pragma unroll
      for (int m = 0; m < 2; ++m) {
#pragma unroll
        for (int s = 0; s < 2; ++s) {
          u32x4 v;
#pragma unroll
          for (int j = 0; j < 4; ++j) v[j] = pk2(acc[m][n][8 * s + 2 * j], acc[m][n][8 * s + 2 * j + 1]);
          st16(base + m * 3072 + s * 768, v);
          if (d == 19) { u32x4 o = {0x3F803F80u, 0x3F803F80u, 0x3F803F80u, 0x3F803F80u}; st16(base + m * 3072 + s * 768 + 16, o); }
        }
      }
    }
  } else {
    const bool isqk = ct < 10;
    const char* Bb = isqk ? (const char*)wqkv + ct * 256 : (const char*)w1 + (ct - 15) * 256;
    const bool isq = ct < 5;
    const unsigned ts = isq ? 1536 : 3072;
    unsigned eoff[4], mixed = 0;
#pragma unroll
    for (int i = 0; i < 4; ++i) {
      const int U = (isq ? ct : ct - 5) * 16 + wc * 8 + 2 * i + h;
      eoff[i] = unit_off(U, 64 * ts) + r * 16;
      mixed |= (U % 5 == 2) ? 1u << i : 0u;
    }
    asm volatile("" : "+v"(eoff[0]), "+v"(eoff[1]), "+v"(eoff[2]), "+v"(eoff[3]), "+v"(mixed));
    gemm8<2, 2, 0, true, K1_NS>(acc, Abase, 0, Bb, isqk ? NQKV * 2 : DFF * 2, ct == 28 ? 7 : 15, 10, lds);
    if (isqk) {
      const float sc = isq ? QSCALE : 1.f;
      char* base = ws + (isq ? WS_Q : WS_KV) + (size_t)tile0 * ts;
#pragma unroll
      for (int n = 0; n < 2; ++n) {
#pragma unroll
        for (int gp = 0; gp < 2; ++gp) {
#pragma unroll
          for (int m = 0; m < 2; ++m) {
            const f32x16& a = acc[m][n];
            unsigned a0 = pk2(a[8 * gp] * sc, a[8 * gp + 1] * sc), a1 = pk2(a[8 * gp + 2] * sc, a[8 * gp + 3] * sc);
            unsigned b0 = pk2(a[8 * gp + 4] * sc, a[8 * gp + 5] * sc), b1 = pk2(a[8 * gp + 6] * sc, a[8 * gp + 7] * sc);
            auto s0 = __builtin_amdgcn_permlane32_swap(a0, b0, false, false);
            auto s1 = __builtin_amdgcn_permlane32_swap(a1, b1, false, false);
            u32x4 v = {s0[0], s1[0], s0[1], s1[1]};
            char* p = base + m * ts + eoff[n * 2 + gp];
            st16(p, v);
            if (mixed >> (n * 2 + gp) & 1) st16(p + 64 * ts, v);
          }
        }
      }
    } else {
#pragma unroll
      for (int n = 0; n < 2; ++n) {
        const int c32 = (ct - 15) * 4 + wc * 2 + n;
        if (c32 < 54) {
#pragma unroll
          for (int m = 0; m < 2; ++m) {
#pragma unroll
            for (int s = 0; s < 2; ++s) {
              u32x4 v;
#pragma unroll
              for (int q = 0; q < 4; ++q) v[q] = relu_pk(pk2(acc[m][n][8 * s + 2 * q], acc[m][n][8 * s + 2 * q + 1]));
              st16(ws + WS_H + ((size_t)((tile0 + m) * 2 + (r >> 4)) * 54 + c32) * 1024 + ((2 * s + h) * 16 + (r & 15)) * 16, v);
            }
          }
        }
      }
    }
  }
}

constexpr int K2_LDS = 81920;


DI float xhalf_max(float m) {
  auto rr = __builtin_amdgcn_permlane32_swap(__float_as_uint(m), __float_as_uint(m), false, false);
  return fmaxf(__uint_as_float(rr[0]), __uint_as_float(rr[1]));
}
DI float max16(const f32x16& s) {
  float a = fmaxf(fmaxf(s[0], s[1]), s[2]), b = fmaxf(fmaxf(s[3], s[4]), s[5]);
  a = fmaxf(fmaxf(a, s[6]), s[7]); b = fmaxf(fmaxf(b, s[8]), s[9]);
  a = fmaxf(fmaxf(a, s[10]), s[11]); b = fmaxf(fmaxf(b, s[12]), s[13]);
  a = fmaxf(fmaxf(a, s[14]), s[15]);
  return fmaxf(a, b);
}
DI void wait_count(gu32* flag, unsigned target) {
  for (unsigned spins = 0; spins < (1u << 18); ++spins) {
    if ((unsigned)__builtin_amdgcn_readfirstlane(__hip_atomic_load(flag, __ATOMIC_RELAXED, __HIP_MEMORY_SCOPE_AGENT)) >= target) break;
    __builtin_amdgcn_s_sleep(2);
  }
}

DI void attn_wg(char* __restrict__ ws, lds_char* lds, int bid) {
  const int tid = threadIdx.x, l = tid & 63;
  const int w = __builtin_amdgcn_readfirstlane(tid >> 6);
  const int r = l & 31, h = l >> 5;
  const int xcd = bid & 7, idx = bid >> 3;
  const int head = xcd * 4 + (idx >> 3), qb = idx & 7;
  const int qt = qb * 8 + w;
  const char* kv = ws + WS_KV + (size_t)head * 64 * 3072;
  const unsigned lds0 = (unsigned)(uintptr_t)lds;
  const char* kvp = kv + (w < 6 ? w : 0) * 1024;
  const unsigned kvdst = lds0 + (w < 6 ? w * 1024 : 24576), kvstep = w < 6 ? 6144 : 0;
  auto dma_kv = [&](int ch, int U) { glds16(kvp + (size_t)ch * 6144, l * 16, kvdst + U * kvstep); };
  const char* qp = ws + WS_Q + (size_t)(head * 64 + qt) * 1536;
  bf16x8 q1;
  u32x2 q2u;
  asm volatile("global_load_dwordx4 %0, %1, %2" : "=v"(q1) : "v"(l * 16), "s"(qp) : "memory");
  asm volatile("global_load_dwordx2 %0, %1, %2" : "=v"(q2u) : "v"(1024 + r * 16 + (head & 1) * 8), "s"(qp) : "memory");
  dma_kv(0, 0); dma_kv(1, 1); dma_kv(2, 2);
  const unsigned k1rd = l * 16, k2rd = h ? 1536 + 20 * 16 : 1024 + r * 16 + (head & 1) * 8;
  const int dd = r < 24 ? r : (r < 28 ? r - 4 : r - 12);
  const unsigned vrd = 1536 + h * 384 + dd * 16;
  f32x16 zero;
#pragma unroll
  for (int i = 0; i < 16; ++i) zero[i] = 0.f;
  asm volatile("s_waitcnt vmcnt(2)" : "+v"(q1), "+v"(q2u)::"memory");
  RAW_BAR();
  if (h) { q2u[0] = 0; q2u[1] = 0; }
  bf16x8 kf1 = LDS128(lds + k1rd);
  bf16x4 kf2 = LDS64(lds + k2rd);
  {
    f32x16 s = MFMA16(kf1, q1, zero);
    s = MFMA8(kf2, __builtin_bit_cast(bf16x4, q2u), s);
    float m = xhalf_max(max16(s));
    if (h) q2u[0] = pk2(-m, 0.f);
  }
  const bf16x4 q2 = __builtin_bit_cast(bf16x4, q2u);
  f32x16 o = zero;
  f32x16 sA = MFMA16(kf1, q1, zero);
  sA = MFMA8(kf2, q2, sA);
  f32x16 sB;
  kf1 = LDS128(lds + 3072 + k1rd);
  kf2 = LDS64(lds + 3072 + k2rd);
  bf16x8 vf0 = LDS128(lds + vrd);
  bf16x8 vf1 = vf0;
  u32x4 p0, p1 = {0u, 0u, 0u, 0u};
#define AT_STEP(SC, SN, KN, VC, VN, D0) do { \
    SN = MFMA16(kf1, q1, zero); \
    kf1 = LDS128((KN) + k1rd); SBAR(); \
    SC[0] = EX(SC[0]); SC[1] = EX(SC[1]); SC[2] = EX(SC[2]); SC[3] = EX(SC[3]); PIN(SC); SBAR(); \
    o = MFMA16(vf1, __builtin_bit_cast(bf16x8, p1), o); PIN(o); \
    vf1 = LDS128((VC) + vrd + 768); SBAR(); \
    D0; \
    SC[4] = EX(SC[4]); SC[5] = EX(SC[5]); SC[6] = EX(SC[6]); SC[7] = EX(SC[7]); \
    p0[0] = pk2(SC[0], SC[1]); p0[1] = pk2(SC[2], SC[3]); p0[2] = pk2(SC[4], SC[5]); p0[3] = pk2(SC[6], SC[7]); PIN(p0); SBAR(); \
    SN = MFMA8(kf2, q2, SN); \
    kf2 = LDS64((KN) + k2rd); SBAR(); \
    SC[8] = EX(SC[8]); SC[9] = EX(SC[9]); SC[10] = EX(SC[10]); SC[11] = EX(SC[11]); PIN(SC); SBAR(); \
    o = MFMA16(vf0, __builtin_bit_cast(bf16x8, p0), o); PIN(o); \
    vf0 = LDS128((VN) + vrd); SBAR(); \
    SC[12] = EX(SC[12]); SC[13] = EX(SC[13]); SC[14] = EX(SC[14]); SC[15] = EX(SC[15]); \
    p1[0] = pk2(SC[8], SC[9]); p1[1] = pk2(SC[10], SC[11]); p1[2] = pk2(SC[12], SC[13]); p1[3] = pk2(SC[14], SC[15]); PIN(p1); SBAR(); \
  } while (0)
#define NOP_ ((void)0)
#define SUPER(U, WN, X0) do { \
    WAIT_VM(WN); \
    RAW_BAR(); \
    AT_STEP(sA, sB, lds + (((U) + 1) & 3) * 6144, lds + (U) * 6144, lds + (U) * 6144 + 3072, X0); \
    AT_STEP(sB, sA, lds + (((U) + 1) & 3) * 6144 + 3072, lds + (U) * 6144 + 3072, lds + (((U) + 1) & 3) * 6144, NOP_); \
  } while (0)
#pragma unroll 1
  for (int c = 0; c < 28; c += 4) {
    SUPER(0, 1, dma_kv(c + 3, 3)); SUPER(1, 1, dma_kv(c + 4, 0)); SUPER(2, 1, dma_kv(c + 5, 1)); SUPER(3, 1, dma_kv(c + 6, 2));
  }
  SUPER(0, 1, dma_kv(31, 3)); SUPER(1, 1, NOP_); SUPER(2, 0, NOP_);
  if (tid == 0) __hip_atomic_fetch_add((gu32*)(ws + WS_FLAG + qb * 256), 1u, __ATOMIC_RELAXED, __HIP_MEMORY_SCOPE_AGENT);
  SUPER(3, 63, NOP_);
  o = MFMA16(vf1, __builtin_bit_cast(bf16x8, p1), o);
  float lsum;
  {
    auto rr = __builtin_amdgcn_permlane32_swap(__float_as_uint(o[8]), __float_as_uint(o[8]), false, false);
    lsum = __uint_as_float(rr[1]);
  }
  bool okv = lsum < 1.0e18f;
#pragma unroll
  for (int i = 0; i < 12; ++i) okv = okv && ((h && i >= 8) ? true : fabsf(o[i]) < 1.0e30f);
  if (!__all(okv)) {
    u32x2 qz = q2u; if (h) qz[0] = 0;
    const bf16x4 q2z = __builtin_bit_cast(bf16x4, qz);
    float m = -INFINITY;
    o = zero;
    for (int t = 0; t < 64; ++t) {
      const char* tb = kv + (size_t)t * 3072;
      bf16x8 k1 = *(const bf16x8*)(tb + k1rd);
      bf16x4 k2 = *(const bf16x4*)(tb + k2rd);
      bf16x8 v0 = *(const bf16x8*)(tb + vrd);
      bf16x8 v1 = *(const bf16x8*)(tb + vrd + 768);
      f32x16 s = MFMA16(k1, q1, zero);
      s = MFMA8(k2, q2z, s);
      float mn = fmaxf(m, xhalf_max(max16(s)));
      float alpha = EX(m - mn);
      m = mn;
#pragma unroll
      for (int i = 0; i < 16; ++i) o[i] *= alpha;
      u32x4 a0, a1;
#pragma unroll
      for (int j = 0; j < 4; ++j) {
        a0[j] = pk2(EX(s[2 * j] - m), EX(s[2 * j + 1] - m));
        a1[j] = pk2(EX(s[8 + 2 * j] - m), EX(s[8 + 2 * j + 1] - m));
      }
      o = MFMA16(v0, __builtin_bit_cast(bf16x8, a0), o);
      o = MFMA16(v1, __builtin_bit_cast(bf16x8, a1), o);
    }
    auto rr = __builtin_amdgcn_permlane32_swap(__float_as_uint(o[8]), __float_as_uint(o[8]), false, false);
    lsum = __uint_as_float(rr[1]);
  }
  const float inv = 1.f / lsum;
  char* ob = ws + WS_O + (size_t)(qt * 2 + (r >> 4)) * 20 * 1024 + (r & 15) * 16;
#pragma unroll
  for (int g = 0; g < 3; ++g) {
    const int gi = 2 * g + h;
    if (gi < 5) {
      const int G = 5 * head + gi;
      u32x2 v;
      v[0] = pk2(o[4 * g] * inv, o[4 * g + 1] * inv);
      v[1] = pk2(o[4 * g + 2] * inv, o[4 * g + 3] * inv);
      __hip_atomic_store((gu64*)(ob + (G >> 3) * 1024 + (G & 3) * 256 + ((G >> 2) & 1) * 8), __builtin_bit_cast(unsigned long long, v),
                         __ATOMIC_RELAXED, __HIP_MEMORY_SCOPE_AGENT);
    }
  }
}

DI void out_wg(const u16* __restrict__ w2, const u16* __restrict__ wo, char* __restrict__ ws, u16* __restrict__ out, lds_char* lds, int j) {
  const int tid = threadIdx.x, l = tid & 63;
  const int w = __builtin_amdgcn_readfirstlane(tid >> 6);
  const unsigned lds0 = (unsigned)(uintptr_t)lds;
  const int wt = w & 3, kh = w >> 2;
  const int rt = j & 31, ct = j >> 5;
  const int tk = l & 15, kq = l >> 4;
  const int token = rt * 64 + wt * 16 + tk;
  lds_char* red = lds + wt * 5120 + l * 16;
  const unsigned roff = (l / 10) * 1280 + (l % 10) * 16;
  constexpr unsigned long long M60 = (1ull << 60) - 1, M40 = (1ull << 40) - 1;
  float bt[3][4];
  const float g = ((const float*)(ws + WS_G))[token];
  {
    const char* wb2 = (const char*)w2 + ct * 160;
    auto wstage = [&](int st, int slot) {
#pragma unroll
      for (int i = 0; i < 4; ++i) glds16m(wb2 + (size_t)(st * 192 + (i * 8 + w) * 6) * 1280, roff, lds0 + slot * 30720 + (i * 8 + w) * 960, M60);
    };
    const bf16x8* hp = (const bf16x8*)(ws + WS_H + (size_t)((rt * 4 + wt) * 54 + kh * 3) * 1024) + l;
    bf16x8 ha[3], hb[3];
#pragma unroll
    for (int i = 0; i < 3; ++i) ha[i] = hp[i * 64];
    wstage(0, 0);
    f32x4 accb[5];
#pragma unroll
    for (int n = 0; n < 5; ++n) accb[n] = f32x4{0.f, 0.f, 0.f, 0.f};
    const unsigned br2 = (kh * 96 + 16 * (kq >> 1) + 4 * (kq & 1) + (tk >> 2)) * 160 + (tk & 3) * 8;
    auto fstage = [&](int st, int slot, bf16x8 (&hc)[3], bf16x8 (&hn)[3], bool more) {
      WAIT_VM(0);
      RAW_BAR();
      if (more) {
#pragma unroll
        for (int i = 0; i < 3; ++i) hn[i] = hp[((st + 1) * 6 + i) * 64];
        wstage(st + 1, slot ^ 1);
      }
      lds_char* sb = lds + slot * 30720 + br2;
#pragma unroll
      for (int i = 0; i < 3; ++i)
#pragma unroll
        for (int n = 0; n < 5; ++n) {
          bf16x4 lo = tr_read(sb + i * 32 * 160 + n * 32);
          bf16x4 hi = tr_read(sb + (i * 32 + 8) * 160 + n * 32);
          accb[n] = MFMAS(__builtin_shufflevector(lo, hi, 0, 1, 2, 3, 4, 5, 6, 7), hc[i], accb[n]);
        }
    };
#pragma unroll 1
    for (int st = 0; st < 8; st += 2) { fstage(st, 0, ha, hb, true); fstage(st + 1, 1, hb, ha, true); }
    fstage(8, 0, ha, hb, false);
    __builtin_amdgcn_s_setprio(1);
    RAW_BAR();
#pragma unroll
    for (int n = 0; n < 5; ++n)
      if ((n < 3) != (kh == 0)) *(__attribute__((address_space(3))) f32x4*)(red + n * 1024) = accb[n];
    RAW_BAR();
    const float omg = rbf(1.f - g);
#pragma unroll
    for (int n = 0; n < 5; ++n)
      if ((n < 3) == (kh == 0)) {
        f32x4 a2 = *(__attribute__((address_space(3))) f32x4*)(red + n * 1024);
#pragma unroll
        for (int i = 0; i < 4; ++i) bt[n % 3][i] = rbf(omg * rbf(accb[n][i] + a2[i]));
      }
    RAW_BAR();
  }
  const char* wb = (const char*)wo + ct * 160;
#pragma unroll
  for (int i = 0; i < 3; ++i) {
    const int pc = i * 8 + w, blk = pc >= 11, pp = pc - blk * 11;
    if (pc < 22) glds16m(wb + (size_t)(blk * 320 + pp * 6) * 1280, roff, lds0 + (blk * 64 + pp * 6) * 160, pp == 10 ? M40 : M60);
  }
  WAIT_VM(0);
  RAW_BAR();
  const unsigned brl = (4 * kq + (tk >> 2)) * 160 + (tk & 3) * 8;
  bf16x8 wf[2][5];
#pragma unroll
  for (int ks = 0; ks < 2; ++ks)
#pragma unroll
    for (int n = 0; n < 5; ++n) {
      bf16x4 lo = tr_read(lds + brl + (kh * 64 + ks * 32) * 160 + n * 32);
      bf16x4 hi = tr_read(lds + brl + (kh * 64 + ks * 32 + 16) * 160 + n * 32);
      wf[ks][n] = __builtin_shufflevector(lo, hi, 0, 1, 2, 3, 4, 5, 6, 7);
    }
  RAW_BAR();
#pragma unroll
  for (int i = 0; i < 11; ++i) {
    const int pc = i * 8 + w, blk = pc >= 43, pp = pc - blk * 43;
    if (pc < 86) glds16m(wb + (size_t)(64 + blk * 320 + pp * 6) * 1280, roff, lds0 + (blk * 256 + pp * 6) * 160, pp == 42 ? M40 : M60);
  }
  if (w == 0) wait_count((gu32*)(ws + WS_FLAG + (rt >> 2) * 256), 32u);
  WAIT_VM(0);
  RAW_BAR();
  __builtin_amdgcn_s_setprio(0);
  const char* op = ws + WS_O + (size_t)((rt * 4 + wt) * 20 + kh * 10) * 1024;
  u32x4 of[10];
  for (unsigned spins = 0; spins < (1u << 16); ++spins) {
    asm volatile("global_load_dwordx4 %0, %10, %11 sc1\n\tglobal_load_dwordx4 %1, %10, %11 offset:1024 sc1\n\t"
                 "global_load_dwordx4 %2, %10, %11 offset:2048 sc1\n\tglobal_load_dwordx4 %3, %10, %11 offset:3072 sc1\n\t"
                 "global_load_dwordx4 %4, %12, %11 sc1\n\tglobal_load_dwordx4 %5, %12, %11 offset:1024 sc1\n\t"
                 "global_load_dwordx4 %6, %12, %11 offset:2048 sc1\n\tglobal_load_dwordx4 %7, %12, %11 offset:3072 sc1\n\t"
                 "global_load_dwordx4 %8, %13, %11 sc1\n\tglobal_load_dwordx4 %9, %13, %11 offset:1024 sc1\n\ts_waitcnt vmcnt(0)"
                 : "=&v"(of[0]), "=&v"(of[1]), "=&v"(of[2]), "=&v"(of[3]), "=&v"(of[4]), "=&v"(of[5]), "=&v"(of[6]), "=&v"(of[7]), "=&v"(of[8]), "=&v"(of[9])
                 : "v"(l * 16), "s"(op), "v"(l * 16 + 4096), "v"(l * 16 + 8192) : "memory");
    unsigned mx = 0;
#pragma unroll
    for (int ks = 0; ks < 10; ++ks) mx = max(max(mx, max(of[ks][0], of[ks][1])), max(of[ks][2], of[ks][3]));
    if (__all(mx != 0xFFFFFFFFu)) break;
    __builtin_amdgcn_s_sleep(4);
  }
  __builtin_amdgcn_s_setprio(3);
  f32x4 acc[5];
#pragma unroll
  for (int n = 0; n < 5; ++n) acc[n] = f32x4{0.f, 0.f, 0.f, 0.f};
#pragma unroll
  for (int ks = 0; ks < 10; ++ks) {
#pragma unroll
    for (int n = 0; n < 5; ++n) acc[n] = MFMAS(wf[ks & 1][n], __builtin_bit_cast(bf16x8, of[ks]), acc[n]);
    if (ks + 2 < 10) {
#pragma unroll
      for (int n = 0; n < 5; ++n) {
        bf16x4 lo = tr_read(lds + brl + (kh * 256 + ks * 32) * 160 + n * 32);
        bf16x4 hi = tr_read(lds + brl + (kh * 256 + ks * 32 + 16) * 160 + n * 32);
        wf[ks & 1][n] = __builtin_shufflevector(lo, hi, 0, 1, 2, 3, 4, 5, 6, 7);
      }
    }
    SBAR();
  }
  RAW_BAR();
#pragma unroll
  for (int n = 0; n < 5; ++n)
    if ((n < 3) != (kh == 0)) *(__attribute__((address_space(3))) f32x4*)(red + n * 1024) = acc[n];
  RAW_BAR();
  const size_t eo = (size_t)token * DM + ct * 80 + 4 * kq;
#pragma unroll
  for (int n = 0; n < 5; ++n) {
    if ((n < 3) == (kh == 0)) {
      f32x4 a2 = *(__attribute__((address_space(3))) f32x4*)(red + n * 1024);
      float v[4];
#pragma unroll
      for (int i = 0; i < 4; ++i) v[i] = rbf(g * rbf(acc[n][i] + a2[i])) + bt[n % 3][i];
      u32x2 o2;
      o2[0] = pk2(v[0], v[1]);
      o2[1] = pk2(v[2], v[3]);
      *(u32x2*)(out + eo + n * 16) = o2;
    }
  }
}

__global__ __launch_bounds__(512, 4) void k2_kernel(const u16* __restrict__ w2, const u16* __restrict__ wo, char* __restrict__ ws,
                                                    u16* __restrict__ out) {
  extern __shared__ __attribute__((aligned(16))) char smem[];
  lds_char* lds = (lds_char*)smem;
  const int bid = blockIdx.x;
  if (bid < 256) attn_wg(ws, lds, bid);
  else { out_wg(w2, wo, ws, out, lds, bid - 256); }
}

extern "C" void kernel_launch(void* const* d_in, const int* in_sizes, int n_in, void* d_out, int out_size, void* d_ws, size_t ws_size,
                              hipStream_t stream) {
  const u16* x = (const u16*)d_in[0];
  const u16* wqkv = (const u16*)d_in[1];
  const u16* wo = (const u16*)d_in[2];
  const u16* w1 = (const u16*)d_in[4];
  const u16* w2 = (const u16*)d_in[5];
  const u16* gw = (const u16*)d_in[6];
  char* ws = (char*)d_ws;
  hipFuncSetAttribute((const void*)k1_kernel, hipFuncAttributeMaxDynamicSharedMemorySize, K1_LDS);
  hipFuncSetAttribute((const void*)k2_kernel, hipFuncAttributeMaxDynamicSharedMemorySize, K2_LDS);
  k1_kernel<<<256, 512, K1_LDS, stream>>>(x, wqkv, w1, gw, ws);
  k2_kernel<<<512, 512, K2_LDS, stream>>>(w2, wo, ws, (u16*)d_out);
}
```

```cpp
#include <hip/hip_runtime.h>
#include <hip/hip_bf16.h>
#include <stdint.h>
#include <type_traits>

typedef short bf16x8 __attribute__((ext_vector_type(8)));
typedef short bf16x4 __attribute__((ext_vector_type(4)));
typedef float f32x16 __attribute__((ext_vector_type(16)));
typedef float f32x4 __attribute__((ext_vector_type(4)));
typedef float f32x2 __attribute__((ext_vector_type(2)));
typedef __bf16 bfx2 __attribute__((ext_vector_type(2)));
typedef unsigned u32x4 __attribute__((ext_vector_type(4)));
typedef unsigned u32x2 __attribute__((ext_vector_type(2)));
typedef __attribute__((address_space(3))) char lds_char;
typedef __attribute__((address_space(1))) unsigned gu32;
typedef __attribute__((address_space(1))) unsigned long long gu64;
typedef unsigned short u16;

#define DI __device__ __forceinline__
#define MFMA16(a, b, c) __builtin_amdgcn_mfma_f32_32x32x16_bf16((a), (b), (c), 0, 0, 0)
#define MFMA8(a, b, c) __builtin_amdgcn_mfma_f32_32x32x8bf16_1k((a), (b), (c), 0, 0, 0)
#define MFMAS(a, b, c) __builtin_amdgcn_mfma_f32_16x16x32_bf16((a), (b), (c), 0, 0, 0)

constexpr int SEQ = 2048, DM = 640, NH = 32, DH = 20, DFF = 1728, NQKV = 1920;

constexpr size_t WS_KV = 0;
constexpr size_t WS_Q = WS_KV + (size_t)32 * 64 * 3072;
constexpr size_t WS_H = WS_Q + (size_t)32 * 64 * 1536;
constexpr size_t WS_O = WS_H + (size_t)64 * 108 * 1024;
constexpr size_t WS_WT = WS_O + (size_t)128 * 20 * 1024;
constexpr size_t WS_G = WS_WT + (size_t)8 * 74 * 5120;
constexpr size_t WS_FLAG = WS_G + 2048 * 4;

DI unsigned pk2(float lo, float hi) {
  f32x2 v = {lo, hi};
  bfx2 r = __builtin_convertvector(v, bfx2);
  return __builtin_bit_cast(unsigned, r);
}
DI void st16(void* p, u32x4 v) { __builtin_nontemporal_store(v, (u32x4*)p); }
DI unsigned relu_pk(unsigned v) {
  typedef short s16x2 __attribute__((ext_vector_type(2)));
  s16x2 a = __builtin_bit_cast(s16x2, v), z = {0, 0};
  return __builtin_bit_cast(unsigned, __builtin_elementwise_max(a, z));
}
DI float rbf(float x) { return (float)(__bf16)x; }
DI float bfbits2f(unsigned short b) { return __uint_as_float((unsigned)b << 16); }

DI void glds16(const char* sbase, unsigned voff, unsigned lds_dst) {
  unsigned keep;
  lds_dst = __builtin_amdgcn_readfirstlane(lds_dst);
  asm volatile("s_mov_b32 %0, m0\n\ts_mov_b32 m0, %3\n\ts_nop 0\n\tglobal_load_lds_dwordx4 %1, %2\n\ts_mov_b32 m0, %0"
               : "=&s"(keep) : "v"(voff), "s"(sbase), "s"(lds_dst) : "memory");
}
DI void glds16m(const char* sbase, unsigned voff, unsigned lds_dst, unsigned long long mask) {
  unsigned keep;
  lds_dst = __builtin_amdgcn_readfirstlane(lds_dst);
  asm volatile("s_mov_b32 %0, m0\n\ts_mov_b32 m0, %3\n\ts_mov_b64 exec, %4\n\tglobal_load_lds_dwordx4 %1, %2\n\ts_mov_b64 exec, -1\n\ts_mov_b32 m0, %0"
               : "=&s"(keep) : "v"(voff), "s"(sbase), "s"(lds_dst), "s"(mask) : "memory");
}
typedef short v4i16_t __attribute__((ext_vector_type(4)));
DI bf16x4 tr_read(lds_char* p) {
  return __builtin_bit_cast(bf16x4, __builtin_amdgcn_ds_read_tr16_b64_v4i16((__attribute__((address_space(3))) v4i16_t*)p));
}
#define SBAR() __builtin_amdgcn_sched_barrier(0)
#define PIN(x) asm volatile("" : "+v"(x))
#define WAIT_VM(n) asm volatile("s_waitcnt vmcnt(%0)" ::"n"(n) : "memory")
#define RAW_BAR() do { asm volatile("" ::: "memory"); __builtin_amdgcn_s_waitcnt(0xC07F); __builtin_amdgcn_s_barrier(); asm volatile("" ::: "memory"); } while (0)
#define LDS128(p) (*(__attribute__((address_space(3))) bf16x8*)(p))
#define LDS64(p) (*(__attribute__((address_space(3))) bf16x4*)(p))
#define EX(x) __builtin_amdgcn_exp2f(x)


template <int MT, int NT> struct Frag { bf16x8 a[MT]; bf16x4 bl[NT], bh[NT]; };

template <int MT, int NT, int AMODE, bool SWAP, int NS>
DI void gemm8(f32x16 (&acc)[MT][NT], const char* Abase, unsigned a_rt_stride, const char* Bbase, unsigned ldb_bytes, int cmax, int nk,
              lds_char* lds) {
  constexpr int BM = 128 * MT, BN = 64 * NT, BK = 64, KS = 4;
  constexpr int ABYTES = BM * BK * 2, BBYTES = BK * BN * 2, STAGE = ABYTES + BBYTES;
  constexpr int APW = ABYTES / 8192, BPW = BBYTES / 8192, NLD = APW + BPW;
  const int tid = threadIdx.x, l = tid & 63;
  const int w = __builtin_amdgcn_readfirstlane(tid >> 6);
  const int wr = w >> 1, wc = w & 1;
  const int r = l & 31, h = l >> 5;
  unsigned aoff[APW];
#pragma unroll
  for (int i = 0; i < APW; ++i) {
    const int pid = APW * w + i;
    if (AMODE == 0) {
      int row = 8 * pid + (l >> 3);
      aoff[i] = row * 1280 + (((l & 7) ^ ((row >> 1) & 7)) * 16);
    } else {
      aoff[i] = (pid / KS) * a_rt_stride + (pid % KS) * 1024 + l * 16;
    }
  }
  constexpr int BROWS = (BN == 128) ? 4 : 8;
  unsigned boff;
  if (BN == 128) {
    int c = (l & 15) ^ ((l >> 4) << 2);
    c = c < cmax ? c : cmax;
    boff = (BROWS * BPW * w + (l >> 4)) * ldb_bytes + c * 16;
  } else {
    int c = (l & 7) ^ (((l >> 4) & 1) << 2);
    c = c < cmax ? c : cmax;
    boff = (BROWS * BPW * w + (l >> 3)) * ldb_bytes + c * 16;
  }
  const unsigned lds0 = (unsigned)(uintptr_t)lds;
  auto piece = [&](int kt, int slot, int i) {
    const unsigned dst = lds0 + slot * STAGE;
    if (i < APW) {
      const char* ap = Abase + (AMODE == 0 ? kt * (BK * 2) : kt * (KS * 1024));
      glds16(ap, aoff[i < APW ? i : 0], dst + (APW * w + i) * 1024);
    } else {
      const char* bp = Bbase + (size_t)kt * BK * ldb_bytes;
      glds16(bp + (i - APW) * BROWS * ldb_bytes, boff, dst + ABYTES + (BPW * w + (i - APW)) * 1024);
    }
  };
  auto issue = [&](int kt, int slot) {
#pragma unroll
    for (int i = 0; i < NLD; ++i) piece(kt, slot, i);
  };
  unsigned ard[KS];
#pragma unroll
  for (int ks = 0; ks < KS; ++ks) {
    if (AMODE == 0) ard[ks] = (wr * (32 * MT) + r) * 128 + (((2 * ks + h) ^ ((r >> 1) & 7)) * 16);
    else ard[ks] = (wr * MT) * 4096 + ks * 1024 + l * 16;
  }
  unsigned brd[NT];
  {
    const int q = (l & 15) >> 2, p = l & 3, cbh = (l >> 4) & 1;
    const int krow = (AMODE == 0 ? 8 * h : 4 * h) + q;
#pragma unroll
    for (int n = 0; n < NT; ++n) {
      int chunk = (wc * NT + n) * 4 + 2 * cbh + (p >> 1);
      if (BN == 128) chunk ^= (q << 2); else chunk ^= (((q >> 1) & 1) << 2);
      brd[n] = ABYTES + krow * (BN * 2) + chunk * 16 + (p & 1) * 8;
    }
  }
  constexpr int TOFF = (AMODE == 0 ? 4 : 8) * (BN * 2);
  auto ldA = [&](Frag<MT, NT>& f, lds_char* sb, int ks, int m) { f.a[m] = LDS128(sb + ard[ks] + m * 4096); };
  auto ldB = [&](Frag<MT, NT>& f, lds_char* sb, int ks, int n) {
    f.bl[n] = tr_read(sb + brd[n] + ks * 16 * (BN * 2));
    f.bh[n] = tr_read(sb + brd[n] + ks * 16 * (BN * 2) + TOFF);
  };
  auto mma = [&](Frag<MT, NT>& f, int m, int n) {
    bf16x8 bb = __builtin_shufflevector(f.bl[n], f.bh[n], 0, 1, 2, 3, 4, 5, 6, 7);
    acc[m][n] = SWAP ? MFMA16(bb, f.a[m], acc[m][n]) : MFMA16(f.a[m], bb, acc[m][n]);
  };
  auto step = [&](Frag<MT, NT>& cur, Frag<MT, NT>& nxt, lds_char* nsb, int nks, bool pf, int pkt, int pslot, int P0, int P1) {
    mma(cur, 0, 0);
    ldB(nxt, nsb, nks, 0); ldA(nxt, nsb, nks, 0);
    SBAR();
    if (NT == 2) { mma(cur, 0, NT - 1); ldB(nxt, nsb, nks, NT - 1); }
    if (MT == 2) ldA(nxt, nsb, nks, MT - 1);
    SBAR();
    if (pf && P0 < P1) piece(pkt, pslot, P0);
    if (MT == 2) { mma(cur, MT - 1, 0); SBAR(); }
    if (pf) { for (int i = P0 + 1; i < P1; ++i) piece(pkt, pslot, i); }
    if (MT == 2 && NT == 2) mma(cur, MT - 1, NT - 1);
    SBAR();
  };
  constexpr int PQ = (NLD + 2) / 3;
  constexpr int P1 = PQ, P2 = 2 * PQ < NLD ? 2 * PQ : NLD;
  issue(0, 0);
  issue(1, 1);
  issue(2, 2);
  Frag<MT, NT> X, Y;
  WAIT_VM(2 * NLD);
  RAW_BAR();
#pragma unroll
  for (int n = 0; n < NT; ++n) ldB(X, lds, 0, n);
#pragma unroll
  for (int m = 0; m < MT; ++m) ldA(X, lds, 0, m);
  int slot = 0, pslot = NS - 1;
  static_assert(NS == 3, "");
  auto body = [&](int t, auto pf_c, auto fl_c) {
    constexpr bool pf = decltype(pf_c)::value, fl = decltype(fl_c)::value;
    lds_char* sb = lds + slot * STAGE;
    const int pkt = t + NS - 1;
    step(X, Y, sb, 1, pf, pkt, pslot, 0, P1);
    step(Y, X, sb, 2, pf, pkt, pslot, P1, P2);
    step(X, Y, sb, 3, pf, pkt, pslot, P2, NLD);
    if (fl) WAIT_VM(NLD); else WAIT_VM(0);
    RAW_BAR();
    pslot = slot;
    slot = (slot + 1 == NS) ? 0 : slot + 1;
    step(Y, X, lds + slot * STAGE, 0, false, 0, 0, 0, 0);
  };
  body(0, std::false_type{}, std::true_type{});
#pragma unroll 1
  for (int t = 1; t < nk - 2; ++t) body(t, std::true_type{}, std::true_type{});
#pragma unroll 1
  for (int t = 0; t < 2; ++t) body(t, std::false_type{}, std::false_type{});
  RAW_BAR();
}

constexpr int K1_NS = 3;
constexpr int K1_LDS = K1_NS * 49152;
constexpr float QSCALE = 0.22360679774997896f * 1.4426950408889634f;

DI unsigned unit_off(int U, unsigned head_stride) {
  const int p = U / 5, ui = U - p * 5;
  return (2 * p + (ui >= 3 ? 1 : 0)) * head_stride + (ui == 2 ? 1024 : (ui >= 3 ? ui - 3 : ui) * 512);
}

__global__ __launch_bounds__(512, 2) void k1_kernel(const u16* __restrict__ x, const u16* __restrict__ wqkv, const u16* __restrict__ w1,
                                                    const u16* __restrict__ w2, const u16* __restrict__ wo, const u16* __restrict__ gw,
                                                    char* __restrict__ ws) {
  extern __shared__ __attribute__((aligned(16))) char smem[];
  lds_char* lds = (lds_char*)smem;
  const int bid = blockIdx.x, tid = threadIdx.x, l = tid & 63;
  const int w = __builtin_amdgcn_readfirstlane(tid >> 6);
  if (bid >= 232) {
    const unsigned lds0 = (unsigned)(uintptr_t)lds;
    const unsigned roff = (l / 10) * 1280 + (l % 10) * 16;
    const int tk = l & 15, kq = l >> 4;
    const int sw = (bid - 232) * 8 + w;
    auto tload = [&](int u, unsigned sb) {
      const int ctt = u / 74, ks = u - ctt * 74;
      const char* src = (ks < 54 ? (const char*)w2 + (size_t)ks * 32 * 1280 : (const char*)wo + (size_t)(ks - 54) * 32 * 1280) + ctt * 160;
#pragma unroll
      for (int pc = 0; pc < 6; ++pc) glds16m(src + pc * 6 * 1280, roff, lds0 + sb + pc * 960, pc == 5 ? (1ull << 20) - 1 : (1ull << 60) - 1);
    };
    auto tstore = [&](int u, unsigned sb) {
      const int ks = u % 74;
      const unsigned rd = sb + ((ks < 54 ? 16 * (kq >> 1) + 4 * (kq & 1) : 4 * kq) + (tk >> 2)) * 160 + (tk & 3) * 8;
      const unsigned hi = (ks < 54 ? 8 : 16) * 160;
#pragma unroll
      for (int n = 0; n < 5; ++n) {
        bf16x4 a = tr_read(lds + rd + n * 32), b = tr_read(lds + rd + hi + n * 32);
        st16(ws + WS_WT + (size_t)u * 5120 + n * 1024 + l * 16, __builtin_bit_cast(u32x4, __builtin_shufflevector(a, b, 0, 1, 2, 3, 4, 5, 6, 7)));
      }
    };
    const bool extra = bid >= 252 && w < 4;
    {
      const u32x4 sn = {0xFFFFFFFFu, 0xFFFFFFFFu, 0xFFFFFFFFu, 0xFFFFFFFFu};
#pragma unroll
      for (int i = 0; i < 14; ++i)
        if (i * 192 + sw < 2560) st16(ws + WS_O + (size_t)(i * 192 + sw) * 1024 + l * 16, sn);
    }
    if (bid < 248) {
#pragma unroll
      for (int half = 0; half < 2; ++half) {
        const int row = (bid - 232) * 128 + half * 64 + w * 8 + (l >> 3), sub = l & 7;
        float s = 0.f;
#pragma unroll
        for (int i = 0; i < 10; ++i) {
          const int c = (i * 8 + sub) * 8;
          u32x4 xv = *(const u32x4*)(x + (size_t)row * DM + c);
          u32x4 gv = *(const u32x4*)(gw + c);
#pragma unroll
          for (int q = 0; q < 4; ++q) {
            s = fmaf(__uint_as_float(xv[q] << 16), __uint_as_float(gv[q] << 16), s);
            s = fmaf(__uint_as_float(xv[q] & 0xFFFF0000u), __uint_as_float(gv[q] & 0xFFFF0000u), s);
          }
        }
        s += __shfl_xor(s, 1); s += __shfl_xor(s, 2); s += __shfl_xor(s, 4);
        s = rbf(s);
        float g = rbf(1.f / (1.f + __expf(-s)));
        if (sub == 0) ((float*)(ws + WS_G))[row] = g;
      }
    }
#pragma unroll
    for (int i = 0; i < 3; ++i) tload(sw * 3 + i, w * 15360 + i * 5120);
    if (extra) tload(576 + (bid - 252) * 4 + w, 122880 + w * 5120);
    WAIT_VM(0);
#pragma unroll
    for (int i = 0; i < 3; ++i) tstore(sw * 3 + i, w * 15360 + i * 5120);
    if (extra) tstore(576 + (bid - 252) * 4 + w, 122880 + w * 5120);
    return;
  }
  const int lin = ((bid & 7) >> 1) * 29 + (bid >> 3);
  const int rowp = (bid & 1) * 4 + (lin & 3), ct = lin >> 2;
  const int wr = w >> 1, wc = w & 1, r = l & 31, h = l >> 5;
  if (bid == 0 && tid < 24) *(unsigned*)(ws + WS_FLAG + tid * 256) = 0u;
  const char* Abase = (const char*)x + (size_t)rowp * 256 * 1280;
  const int tile0 = rowp * 8 + wr * 2;
  f32x16 acc[2][2];
#pragma unroll
  for (int m = 0; m < 2; ++m)
#pragma unroll
    for (int n = 0; n < 2; ++n)
#pragma unroll
      for (int i = 0; i < 16; ++i) acc[m][n][i] = 0.f;
  if (ct >= 10 && ct < 15) {
    gemm8<2, 2, 0, false, K1_NS>(acc, Abase, 0, (const char*)wqkv + ct * 256, NQKV * 2, 15, 10, lds);
#pragma unroll
    for (int n = 0; n < 2; ++n) {
      const int f = (ct - 10) * 128 + wc * 64 + n * 32 + r;
      const int head = f / 20, d = f - head * 20;
      char* base = ws + WS_KV + (size_t)(head * 64 + tile0) * 3072 + 1536 + h * 384 + d * 16;
#pragma unroll
      for (int m = 0; m < 2; ++m) {
#pragma unroll
        for (int s = 0; s < 2; ++s) {
          u32x4 v;
#pragma unroll
          for (int j = 0; j < 4; ++j) v[j] = pk2(acc[m][n][8 * s + 2 * j], acc[m][n][8 * s + 2 * j + 1]);
          st16(base + m * 3072 + s * 768, v);
          if (d == 19) { u32x4 o = {0x3F803F80u, 0x3F803F80u, 0x3F803F80u, 0x3F803F80u}; st16(base + m * 3072 + s * 768 + 16, o); }
        }
      }
    }
  } else {
    const bool isqk = ct < 10;
    const char* Bb = isqk ? (const char*)wqkv + ct * 256 : (const char*)w1 + (ct - 15) * 256;
    const bool isq = ct < 5;
    const unsigned ts = isq ? 1536 : 3072;
    unsigned eoff[4], mixed = 0;
#pragma unroll
    for (int i = 0; i < 4; ++i) {
      const int U = (isq ? ct : ct - 5) * 16 + wc * 8 + 2 * i + h;
      eoff[i] = unit_off(U, 64 * ts) + r * 16;
      mixed |= (U % 5 == 2) ? 1u << i : 0u;
    }
    asm volatile("" : "+v"(eoff[0]), "+v"(eoff[1]), "+v"(eoff[2]), "+v"(eoff[3]), "+v"(mixed));
    gemm8<2, 2, 0, true, K1_NS>(acc, Abase, 0, Bb, isqk ? NQKV * 2 : DFF * 2, ct == 28 ? 7 : 15, 10, lds);
    if (isqk) {
      const float sc = isq ? QSCALE : 1.f;
      char* base = ws + (isq ? WS_Q : WS_KV) + (size_t)tile0 * ts;
#pragma unroll
      for (int n = 0; n < 2; ++n) {
#pragma unroll
        for (int gp = 0; gp < 2; ++gp) {
#pragma unroll
          for (int m = 0; m < 2; ++m) {
            const f32x16& a = acc[m][n];
            unsigned a0 = pk2(a[8 * gp] * sc, a[8 * gp + 1] * sc), a1 = pk2(a[8 * gp + 2] * sc, a[8 * gp + 3] * sc);
            unsigned b0 = pk2(a[8 * gp + 4] * sc, a[8 * gp + 5] * sc), b1 = pk2(a[8 * gp + 6] * sc, a[8 * gp + 7] * sc);
            auto s0 = __builtin_amdgcn_permlane32_swap(a0, b0, false, false);
            auto s1 = __builtin_amdgcn_permlane32_swap(a1, b1, false, false);
            u32x4 v = {s0[0], s1[0], s0[1], s1[1]};
            char* p = base + m * ts + eoff[n * 2 + gp];
            st16(p, v);
            if (mixed >> (n * 2 + gp) & 1) st16(p + 64 * ts, v);
          }
        }
      }
    } else {
#pragma unroll
      for (int n = 0; n < 2; ++n) {
        const int c32 = (ct - 15) * 4 + wc * 2 + n;
        if (c32 < 54) {
#pragma unroll
          for (int m = 0; m < 2; ++m) {
#pragma unroll
            for (int s = 0; s < 2; ++s) {
              u32x4 v;
#pragma unroll
              for (int q = 0; q < 4; ++q) v[q] = relu_pk(pk2(acc[m][n][8 * s + 2 * q], acc[m][n][8 * s + 2 * q + 1]));
              st16(ws + WS_H + ((size_t)((tile0 + m) * 2 + (r >> 4)) * 54 + c32) * 1024 + ((2 * s + h) * 16 + (r & 15)) * 16, v);
            }
          }
        }
      }
    }
  }
}

constexpr int K2_LDS = 81920;


DI float xhalf_max(float m) {
  auto rr = __builtin_amdgcn_permlane32_swap(__float_as_uint(m), __float_as_uint(m), false, false);
  return fmaxf(__uint_as_float(rr[0]), __uint_as_float(rr[1]));
}
DI float max16(const f32x16& s) {
  float a = fmaxf(fmaxf(s[0], s[1]), s[2]), b = fmaxf(fmaxf(s[3], s[4]), s[5]);
  a = fmaxf(fmaxf(a, s[6]), s[7]); b = fmaxf(fmaxf(b, s[8]), s[9]);
  a = fmaxf(fmaxf(a, s[10]), s[11]); b = fmaxf(fmaxf(b, s[12]), s[13]);
  a = fmaxf(fmaxf(a, s[14]), s[15]);
  return fmaxf(a, b);
}
DI void wait_count(gu32* flag, unsigned target) {
  for (unsigned spins = 0; spins < (1u << 18); ++spins) {
    if ((unsigned)__builtin_amdgcn_readfirstlane(__hip_atomic_load(flag, __ATOMIC_RELAXED, __HIP_MEMORY_SCOPE_AGENT)) >= target) break;
    __builtin_amdgcn_s_sleep(2);
  }
}

DI void attn_wg(char* __restrict__ ws, lds_char* lds, int bid) {
  const int tid = threadIdx.x, l = tid & 63;
  const int w = __builtin_amdgcn_readfirstlane(tid >> 6);
  const int r = l & 31, h = l >> 5;
  const int xcd = bid & 7, idx = bid >> 3;
  const int head = xcd * 4 + (idx >> 3), qb = idx & 7;
  const int qt = qb * 8 + w;
  const char* kv = ws + WS_KV + (size_t)head * 64 * 3072;
  const unsigned lds0 = (unsigned)(uintptr_t)lds;
  const char* kvp = kv + (w < 6 ? w : 0) * 1024;
  const unsigned kvdst = lds0 + (w < 6 ? w * 1024 : 24576), kvstep = w < 6 ? 6144 : 0;
  auto dma_kv = [&](int ch, int U) { glds16(kvp + (size_t)ch * 6144, l * 16, kvdst + U * kvstep); };
  const char* qp = ws + WS_Q + (size_t)(head * 64 + qt) * 1536;
  bf16x8 q1;
  u32x2 q2u;
  asm volatile("global_load_dwordx4 %0, %1, %2" : "=v"(q1) : "v"(l * 16), "s"(qp) : "memory");
  asm volatile("global_load_dwordx2 %0, %1, %2" : "=v"(q2u) : "v"(1024 + r * 16 + (head & 1) * 8), "s"(qp) : "memory");
  dma_kv(0, 0); dma_kv(1, 1); dma_kv(2, 2);
  const unsigned k1rd = l * 16, k2rd = h ? 1536 + 20 * 16 : 1024 + r * 16 + (head & 1) * 8;
  const int dd = r < 24 ? r : (r < 28 ? r - 4 : r - 12);
  const unsigned vrd = 1536 + h * 384 + dd * 16;
  f32x16 zero;
#pragma unroll
  for (int i = 0; i < 16; ++i) zero[i] = 0.f;
  asm volatile("s_waitcnt vmcnt(2)" : "+v"(q1), "+v"(q2u)::"memory");
  RAW_BAR();
  if (h) { q2u[0] = 0; q2u[1] = 0; }
  bf16x8 kf1 = LDS128(lds + k1rd);
  bf16x4 kf2 = LDS64(lds + k2rd);
  {
    f32x16 s = MFMA16(kf1, q1, zero);
    s = MFMA8(kf2, __builtin_bit_cast(bf16x4, q2u), s);
    float m = xhalf_max(max16(s));
    if (h) q2u[0] = pk2(-m, 0.f);
  }
  const bf16x4 q2 = __builtin_bit_cast(bf16x4, q2u);
  f32x16 o = zero;
  f32x16 sA = MFMA16(kf1, q1, zero);
  sA = MFMA8(kf2, q2, sA);
  f32x16 sB;
  kf1 = LDS128(lds + 3072 + k1rd);
  kf2 = LDS64(lds + 3072 + k2rd);
  bf16x8 vf0 = LDS128(lds + vrd);
  bf16x8 vf1 = vf0;
  u32x4 p0, p1 = {0u, 0u, 0u, 0u};
#define AT_STEP(SC, SN, KN, VC, VN, D0) do { \
    SN = MFMA16(kf1, q1, zero); \
    kf1 = LDS128((KN) + k1rd); SBAR(); \
    SC[0] = EX(SC[0]); SC[1] = EX(SC[1]); SC[2] = EX(SC[2]); SC[3] = EX(SC[3]); PIN(SC); SBAR(); \
    o = MFMA16(vf1, __builtin_bit_cast(bf16x8, p1), o); PIN(o); \
    vf1 = LDS128((VC) + vrd + 768); SBAR(); \
    D0; \
    SC[4] = EX(SC[4]); SC[5] = EX(SC[5]); SC[6] = EX(SC[6]); SC[7] = EX(SC[7]); \
    p0[0] = pk2(SC[0], SC[1]); p0[1] = pk2(SC[2], SC[3]); p0[2] = pk2(SC[4], SC[5]); p0[3] = pk2(SC[6], SC[7]); PIN(p0); SBAR(); \
    SN = MFMA8(kf2, q2, SN); \
    kf2 = LDS64((KN) + k2rd); SBAR(); \
    SC[8] = EX(SC[8]); SC[9] = EX(SC[9]); SC[10] = EX(SC[10]); SC[11] = EX(SC[11]); PIN(SC); SBAR(); \
    o = MFMA16(vf0, __builtin_bit_cast(bf16x8, p0), o); PIN(o); \
    vf0 = LDS128((VN) + vrd); SBAR(); \
    SC[12] = EX(SC[12]); SC[13] = EX(SC[13]); SC[14] = EX(SC[14]); SC[15] = EX(SC[15]); \
    p1[0] = pk2(SC[8], SC[9]); p1[1] = pk2(SC[10], SC[11]); p1[2] = pk2(SC[12], SC[13]); p1[3] = pk2(SC[14], SC[15]); PIN(p1); SBAR(); \
  } while (0)
#define NOP_ ((void)0)
#define SUPER(U, WN, X0) do { \
    WAIT_VM(WN); \
    RAW_BAR(); \
    AT_STEP(sA, sB, lds + (((U) + 1) & 3) * 6144, lds + (U) * 6144, lds + (U) * 6144 + 3072, X0); \
    AT_STEP(sB, sA, lds + (((U) + 1) & 3) * 6144 + 3072, lds + (U) * 6144 + 3072, lds + (((U) + 1) & 3) * 6144, NOP_); \
  } while (0)
#pragma unroll 1
  for (int c = 0; c < 28; c += 4) {
    SUPER(0, 1, dma_kv(c + 3, 3)); SUPER(1, 1, dma_kv(c + 4, 0)); SUPER(2, 1, dma_kv(c + 5, 1)); SUPER(3, 1, dma_kv(c + 6, 2));
  }
  SUPER(0, 1, dma_kv(31, 3)); SUPER(1, 1, NOP_); SUPER(2, 0, NOP_);
  if (tid == 0) __hip_atomic_fetch_add((gu32*)(ws + WS_FLAG + qb * 256), 1u, __ATOMIC_RELAXED, __HIP_MEMORY_SCOPE_AGENT);
  SUPER(3, 63, NOP_);
  o = MFMA16(vf1, __builtin_bit_cast(bf16x8, p1), o);
  float lsum;
  {
    auto rr = __builtin_amdgcn_permlane32_swap(__float_as_uint(o[8]), __float_as_uint(o[8]), false, false);
    lsum = __uint_as_float(rr[1]);
  }
  bool okv = lsum < 1.0e18f;
#pragma unroll
  for (int i = 0; i < 12; ++i) okv = okv && ((h && i >= 8) ? true : fabsf(o[i]) < 1.0e30f);
  if (!__all(okv)) {
    u32x2 qz = q2u; if (h) qz[0] = 0;
    const bf16x4 q2z = __builtin_bit_cast(bf16x4, qz);
    float m = -INFINITY;
    o = zero;
    for (int t = 0; t < 64; ++t) {
      const char* tb = kv + (size_t)t * 3072;
      bf16x8 k1 = *(const bf16x8*)(tb + k1rd);
      bf16x4 k2 = *(const bf16x4*)(tb + k2rd);
      bf16x8 v0 = *(const bf16x8*)(tb + vrd);
      bf16x8 v1 = *(const bf16x8*)(tb + vrd + 768);
      f32x16 s = MFMA16(k1, q1, zero);
      s = MFMA8(k2, q2z, s);
      float mn = fmaxf(m, xhalf_max(max16(s)));
      float alpha = EX(m - mn);
      m = mn;
#pragma unroll
      for (int i = 0; i < 16; ++i) o[i] *= alpha;
      u32x4 a0, a1;
#pragma unroll
      for (int j = 0; j < 4; ++j) {
        a0[j] = pk2(EX(s[2 * j] - m), EX(s[2 * j + 1] - m));
        a1[j] = pk2(EX(s[8 + 2 * j] - m), EX(s[8 + 2 * j + 1] - m));
      }
      o = MFMA16(v0, __builtin_bit_cast(bf16x8, a0), o);
      o = MFMA16(v1, __builtin_bit_cast(bf16x8, a1), o);
    }
    auto rr = __builtin_amdgcn_permlane32_swap(__float_as_uint(o[8]), __float_as_uint(o[8]), false, false);
    lsum = __uint_as_float(rr[1]);
  }
  const float inv = 1.f / lsum;
  char* ob = ws + WS_O + (size_t)(qt * 2 + (r >> 4)) * 20 * 1024 + (r & 15) * 16;
#pragma unroll
  for (int g = 0; g < 3; ++g) {
    const int gi = 2 * g + h;
    if (gi < 5) {
      const int G = 5 * head + gi;
      u32x2 v;
      v[0] = pk2(o[4 * g] * inv, o[4 * g + 1] * inv);
      v[1] = pk2(o[4 * g + 2] * inv, o[4 * g + 3] * inv);
      __hip_atomic_store((gu64*)(ob + (G >> 3) * 1024 + (G & 3) * 256 + ((G >> 2) & 1) * 8), __builtin_bit_cast(unsigned long long, v),
                         __ATOMIC_RELAXED, __HIP_MEMORY_SCOPE_AGENT);
    }
  }
}

DI void out_wg(char* __restrict__ ws, u16* __restrict__ out, lds_char* lds, int j) {
  const int tid = threadIdx.x, l = tid & 63;
  const int w = __builtin_amdgcn_readfirstlane(tid >> 6);
  const unsigned lds0 = (unsigned)(uintptr_t)lds;
  const int wt = w & 3, kh = w >> 2;
  const int rt = j & 31, ct = j >> 5;
  const int tk = l & 15, kq = l >> 4;
  const int token = rt * 64 + wt * 16 + tk;
  lds_char* red = lds + wt * 5120 + l * 16;
  const char* wt0 = ws + WS_WT + (size_t)ct * 74 * 5120;
  float bt[3][4];
  const float g = ((const float*)(ws + WS_G))[token];
  {
    auto wstage = [&](int st, int slot) {
#pragma unroll
      for (int i = 0; i < 4; ++i)
        if (i * 8 + w < 30) glds16(wt0 + (size_t)st * 30720 + (i * 8 + w) * 1024, l * 16, lds0 + slot * 30720 + (i * 8 + w) * 1024);
    };
    const bf16x8* hp = (const bf16x8*)(ws + WS_H + (size_t)((rt * 4 + wt) * 54 + kh * 3) * 1024) + l;
    bf16x8 ha[3], hb[3];
#pragma unroll
    for (int i = 0; i < 3; ++i) ha[i] = hp[i * 64];
    wstage(0, 0);
    f32x4 accb[5];
#pragma unroll
    for (int n = 0; n < 5; ++n) accb[n] = f32x4{0.f, 0.f, 0.f, 0.f};
    const unsigned br2 = kh * 15360 + l * 16;
    auto fstage = [&](int st, int slot, bf16x8 (&hc)[3], bf16x8 (&hn)[3], bool more) {
      WAIT_VM(0);
      RAW_BAR();
      if (more) {
#pragma unroll
        for (int i = 0; i < 3; ++i) hn[i] = hp[((st + 1) * 6 + i) * 64];
        wstage(st + 1, slot ^ 1);
      }
      lds_char* sb = lds + slot * 30720 + br2;
#pragma unroll
      for (int i = 0; i < 3; ++i)
#pragma unroll
        for (int n = 0; n < 5; ++n) accb[n] = MFMAS(LDS128(sb + (i * 5 + n) * 1024), hc[i], accb[n]);
    };
#pragma unroll 1
    for (int st = 0; st < 8; st += 2) { fstage(st, 0, ha, hb, true); fstage(st + 1, 1, hb, ha, true); }
    fstage(8, 0, ha, hb, false);
    RAW_BAR();
#pragma unroll
    for (int n = 0; n < 5; ++n)
      if ((n < 3) != (kh == 0)) *(__attribute__((address_space(3))) f32x4*)(red + n * 1024) = accb[n];
    RAW_BAR();
    const float omg = rbf(1.f - g);
#pragma unroll
    for (int n = 0; n < 5; ++n)
      if ((n < 3) == (kh == 0)) {
        f32x4 a2 = *(__attribute__((address_space(3))) f32x4*)(red + n * 1024);
#pragma unroll
        for (int i = 0; i < 4; ++i) bt[n % 3][i] = rbf(omg * rbf(accb[n][i] + a2[i]));
      }
    RAW_BAR();
  }
  const char* wof = wt0 + 54 * 5120;
  bf16x8 wf[2][5];
#pragma unroll
  for (int ks = 0; ks < 2; ++ks)
#pragma unroll
    for (int n = 0; n < 5; ++n) wf[ks][n] = *(const bf16x8*)(wof + ((kh * 10 + ks) * 5 + n) * 1024 + l * 16);
#pragma unroll
  for (int ks = 0; ks < 2; ++ks)
#pragma unroll
    for (int n = 0; n < 5; ++n) PIN(wf[ks][n]);
#pragma unroll
  for (int i = 0; i < 10; ++i) {
    const int pc = i * 8 + w;
    glds16(wof + (pc + (pc < 40 ? 10 : 20)) * 1024, l * 16, lds0 + pc * 1024);
  }
  if (w == 0) wait_count((gu32*)(ws + WS_FLAG + (rt >> 2) * 256), 32u);
  WAIT_VM(0);
  RAW_BAR();
  __builtin_amdgcn_s_setprio(0);
  const char* op = ws + WS_O + (size_t)((rt * 4 + wt) * 20 + kh * 10) * 1024;
  u32x4 of[10];
  for (unsigned spins = 0; spins < (1u << 16); ++spins) {
    asm volatile("global_load_dwordx4 %0, %10, %11 sc1\n\tglobal_load_dwordx4 %1, %10, %11 offset:1024 sc1\n\t"
                 "global_load_dwordx4 %2, %10, %11 offset:2048 sc1\n\tglobal_load_dwordx4 %3, %10, %11 offset:3072 sc1\n\t"
                 "global_load_dwordx4 %4, %12, %11 sc1\n\tglobal_load_dwordx4 %5, %12, %11 offset:1024 sc1\n\t"
                 "global_load_dwordx4 %6, %12, %11 offset:2048 sc1\n\tglobal_load_dwordx4 %7, %12, %11 offset:3072 sc1\n\t"
                 "global_load_dwordx4 %8, %13, %11 sc1\n\tglobal_load_dwordx4 %9, %13, %11 offset:1024 sc1\n\ts_waitcnt vmcnt(0)"
                 : "=&v"(of[0]), "=&v"(of[1]), "=&v"(of[2]), "=&v"(of[3]), "=&v"(of[4]), "=&v"(of[5]), "=&v"(of[6]), "=&v"(of[7]), "=&v"(of[8]), "=&v"(of[9])
                 : "v"(l * 16), "s"(op), "v"(l * 16 + 4096), "v"(l * 16 + 8192) : "memory");
    unsigned mx = 0;
#pragma unroll
    for (int ks = 0; ks < 10; ++ks) mx = max(max(mx, max(of[ks][0], of[ks][1])), max(of[ks][2], of[ks][3]));
    if (__all(mx != 0xFFFFFFFFu)) break;
    __builtin_amdgcn_s_sleep(4);
  }
  __builtin_amdgcn_s_setprio(3);
  f32x4 acc[5];
#pragma unroll
  for (int n = 0; n < 5; ++n) acc[n] = f32x4{0.f, 0.f, 0.f, 0.f};
#pragma unroll
  for (int ks = 0; ks < 10; ++ks) {
#pragma unroll
    for (int n = 0; n < 5; ++n) acc[n] = MFMAS(wf[ks & 1][n], __builtin_bit_cast(bf16x8, of[ks]), acc[n]);
    if (ks + 2 < 10) {
#pragma unroll
      for (int n = 0; n < 5; ++n) wf[ks & 1][n] = LDS128(lds + ((kh * 8 + ks) * 5 + n) * 1024 + l * 16);
    }
    SBAR();
  }
  RAW_BAR();
#pragma unroll
  for (int n = 0; n < 5; ++n)
    if ((n < 3) != (kh == 0)) *(__attribute__((address_space(3))) f32x4*)(red + n * 1024) = acc[n];
  RAW_BAR();
  const size_t eo = (size_t)token * DM + ct * 80 + 4 * kq;
#pragma unroll
  for (int n = 0; n < 5; ++n) {
    if ((n < 3) == (kh == 0)) {
      f32x4 a2 = *(__attribute__((address_space(3))) f32x4*)(red + n * 1024);
      float v[4];
#pragma unroll
      for (int i = 0; i < 4; ++i) v[i] = rbf(g * rbf(acc[n][i] + a2[i])) + bt[n % 3][i];
      u32x2 o2;
      o2[0] = pk2(v[0], v[1]);
      o2[1] = pk2(v[2], v[3]);
      *(u32x2*)(out + eo + n * 16) = o2;
    }
  }
}

__global__ __launch_bounds__(512, 4) void k2_kernel(char* __restrict__ ws, u16* __restrict__ out) {
  extern __shared__ __attribute__((aligned(16))) char smem[];
  lds_char* lds = (lds_char*)smem;
  const int bid = blockIdx.x;
  if (bid < 256) attn_wg(ws, lds, bid);
  else out_wg(ws, out, lds, bid - 256);
}

extern "C" void kernel_launch(void* const* d_in, const int* in_sizes, int n_in, void* d_out, int out_size, void* d_ws, size_t ws_size,
                              hipStream_t stream) {
  const u16* x = (const u16*)d_in[0];
  const u16* wqkv = (const u16*)d_in[1];
  const u16* wo = (const u16*)d_in[2];
  const u16* w1 = (const u16*)d_in[4];
  const u16* w2 = (const u16*)d_in[5];
  const u16* gw = (const u16*)d_in[6];
  char* ws = (char*)d_ws;
  hipFuncSetAttribute((const void*)k1_kernel, hipFuncAttributeMaxDynamicSharedMemorySize, K1_LDS);
  hipFuncSetAttribute((const void*)k2_kernel, hipFuncAttributeMaxDynamicSharedMemorySize, K2_LDS);
  k1_kernel<<<256, 512, K1_LDS, stream>>>(x, wqkv, w1, w2, wo, gw, ws);
  k2_kernel<<<512, 512, K2_LDS, stream>>>(ws, (u16*)d_out);
}
```

```cpp
#include <hip/hip_runtime.h>
#include <hip/hip_bf16.h>
#include <stdint.h>
#include <type_traits>

typedef short bf16x8 __attribute__((ext_vector_type(8)));
typedef short bf16x4 __attribute__((ext_vector_type(4)));
typedef float f32x16 __attribute__((ext_vector_type(16)));
typedef float f32x4 __attribute__((ext_vector_type(4)));
typedef float f32x2 __attribute__((ext_vector_type(2)));
typedef __bf16 bfx2 __attribute__((ext_vector_type(2)));
typedef unsigned u32x4 __attribute__((ext_vector_type(4)));
typedef unsigned u32x2 __attribute__((ext_vector_type(2)));
typedef __attribute__((address_space(3))) char lds_char;
typedef __attribute__((address_space(1))) unsigned gu32;
typedef __attribute__((address_space(1))) unsigned long long gu64;
typedef unsigned short u16;

#define DI __device__ __forceinline__
#define MFMA16(a, b, c) __builtin_amdgcn_mfma_f32_32x32x16_bf16((a), (b), (c), 0, 0, 0)
#define MFMA8(a, b, c) __builtin_amdgcn_mfma_f32_32x32x8bf16_1k((a), (b), (c), 0, 0, 0)
#define MFMAS(a, b, c) __builtin_amdgcn_mfma_f32_16x16x32_bf16((a), (b), (c), 0, 0, 0)

constexpr int SEQ = 2048, DM = 640, NH = 32, DH = 20, DFF = 1728, NQKV = 1920;

constexpr size_t WS_KV = 0;
constexpr unsigned HPAD = 1280;
constexpr unsigned KV_HS = 64 * 3072 + HPAD, Q_HS = 64 * 1536 + HPAD;
constexpr size_t WS_Q = WS_KV + (size_t)32 * KV_HS;
constexpr size_t WS_H = WS_Q + (size_t)32 * Q_HS;
constexpr size_t WS_O = WS_H + (size_t)64 * 108 * 1024;
constexpr size_t WS_WT = WS_O + (size_t)128 * 20 * 1024;
constexpr size_t WS_G = WS_WT + (size_t)8 * 74 * 5120;
constexpr size_t WS_FLAG = WS_G + 2048 * 4;

DI unsigned pk2(float lo, float hi) {
  f32x2 v = {lo, hi};
  bfx2 r = __builtin_convertvector(v, bfx2);
  return __builtin_bit_cast(unsigned, r);
}
DI void st16(void* p, u32x4 v) { asm volatile("global_store_dwordx4 %0, %1, off sc1\n\ts_nop 1" :: "v"(p), "v"(v) : "memory"); }
DI unsigned relu_pk(unsigned v) {
  typedef short s16x2 __attribute__((ext_vector_type(2)));
  s16x2 a = __builtin_bit_cast(s16x2, v), z = {0, 0};
  return __builtin_bit_cast(unsigned, __builtin_elementwise_max(a, z));
}
DI float rbf(float x) { return (float)(__bf16)x; }
DI float bfbits2f(unsigned short b) { return __uint_as_float((unsigned)b << 16); }

DI void glds16(const char* sbase, unsigned voff, unsigned lds_dst) {
  unsigned keep;
  lds_dst = __builtin_amdgcn_readfirstlane(lds_dst);
  asm volatile("s_mov_b32 %0, m0\n\ts_mov_b32 m0, %3\n\ts_nop 0\n\tglobal_load_lds_dwordx4 %1, %2\n\ts_mov_b32 m0, %0"
               : "=&s"(keep) : "v"(voff), "s"(sbase), "s"(lds_dst) : "memory");
}
DI void glds16m(const char* sbase, unsigned voff, unsigned lds_dst, unsigned long long mask) {
  unsigned keep;
  lds_dst = __builtin_amdgcn_readfirstlane(lds_dst);
  asm volatile("s_mov_b32 %0, m0\n\ts_mov_b32 m0, %3\n\ts_mov_b64 exec, %4\n\tglobal_load_lds_dwordx4 %1, %2\n\ts_mov_b64 exec, -1\n\ts_mov_b32 m0, %0"
               : "=&s"(keep) : "v"(voff), "s"(sbase), "s"(lds_dst), "s"(mask) : "memory");
}
typedef short v4i16_t __attribute__((ext_vector_type(4)));
DI bf16x4 tr_read(lds_char* p) {
  return __builtin_bit_cast(bf16x4, __builtin_amdgcn_ds_read_tr16_b64_v4i16((__attribute__((address_space(3))) v4i16_t*)p));
}
#define SBAR() __builtin_amdgcn_sched_barrier(0)
#define PIN(x) asm volatile("" : "+v"(x))
#define WAIT_VM(n) asm volatile("s_waitcnt vmcnt(%0)" ::"n"(n) : "memory")
#define RAW_BAR() do { asm volatile("" ::: "memory"); __builtin_amdgcn_s_waitcnt(0xC07F); __builtin_amdgcn_s_barrier(); asm volatile("" ::: "memory"); } while (0)
#define LDS128(p) (*(__attribute__((address_space(3))) bf16x8*)(p))
#define LDS64(p) (*(__attribute__((address_space(3))) bf16x4*)(p))
#define EX(x) __builtin_amdgcn_exp2f(x)


template <int MT, int NT> struct Frag { bf16x8 a[MT]; bf16x4 bl[NT], bh[NT]; };

template <int MT, int NT, int AMODE, bool SWAP, int NS>
DI void gemm8(f32x16 (&acc)[MT][NT], const char* Abase, unsigned a_rt_stride, const char* Bbase, unsigned ldb_bytes, int cmax, int nk,
              lds_char* lds) {
  constexpr int BM = 128 * MT, BN = 64 * NT, BK = 64, KS = 4;
  constexpr int ABYTES = BM * BK * 2, BBYTES = BK * BN * 2, STAGE = ABYTES + BBYTES;
  constexpr int APW = ABYTES / 8192, BPW = BBYTES / 8192, NLD = APW + BPW;
  const int tid = threadIdx.x, l = tid & 63;
  const int w = __builtin_amdgcn_readfirstlane(tid >> 6);
  const int wr = w >> 1, wc = w & 1;
  const int r = l & 31, h = l >> 5;
  unsigned aoff[APW];
#pragma unroll
  for (int i = 0; i < APW; ++i) {
    const int pid = APW * w + i;
    if (AMODE == 0) {
      int row = 8 * pid + (l >> 3);
      aoff[i] = row * 1280 + (((l & 7) ^ ((row >> 1) & 7)) * 16);
    } else {
      aoff[i] = (pid / KS) * a_rt_stride + (pid % KS) * 1024 + l * 16;
    }
  }
  constexpr int BROWS = (BN == 128) ? 4 : 8;
  unsigned boff;
  if (BN == 128) {
    int c = (l & 15) ^ ((l >> 4) << 2);
    c = c < cmax ? c : cmax;
    boff = (BROWS * BPW * w + (l >> 4)) * ldb_bytes + c * 16;
  } else {
    int c = (l & 7) ^ (((l >> 4) & 1) << 2);
    c = c < cmax ? c : cmax;
    boff = (BROWS * BPW * w + (l >> 3)) * ldb_bytes + c * 16;
  }
  const unsigned lds0 = (unsigned)(uintptr_t)lds;
  auto piece = [&](int kt, int slot, int i) {
    const unsigned dst = lds0 + slot * STAGE;
    if (i < APW) {
      const char* ap = Abase + (AMODE == 0 ? kt * (BK * 2) : kt * (KS * 1024));
      glds16(ap, aoff[i < APW ? i : 0], dst + (APW * w + i) * 1024);
    } else {
      const char* bp = Bbase + (size_t)kt * BK * ldb_bytes;
      glds16(bp + (i - APW) * BROWS * ldb_bytes, boff, dst + ABYTES + (BPW * w + (i - APW)) * 1024);
    }
  };
  auto issue = [&](int kt, int slot) {
#pragma unroll
    for (int i = 0; i < NLD; ++i) piece(kt, slot, i);
  };
  unsigned ard[KS];
#pragma unroll
  for (int ks = 0; ks < KS; ++ks) {
    if (AMODE == 0) ard[ks] = (wr * (32 * MT) + r) * 128 + (((2 * ks + h) ^ ((r >> 1) & 7)) * 16);
    else ard[ks] = (wr * MT) * 4096 + ks * 1024 + l * 16;
  }
  unsigned brd[NT];
  {
    const int q = (l & 15) >> 2, p = l & 3, cbh = (l >> 4) & 1;
    const int krow = (AMODE == 0 ? 8 * h : 4 * h) + q;
#pragma unroll
    for (int n = 0; n < NT; ++n) {
      int chunk = (wc * NT + n) * 4 + 2 * cbh + (p >> 1);
      if (BN == 128) chunk ^= (q << 2); else chunk ^= (((q >> 1) & 1) << 2);
      brd[n] = ABYTES + krow * (BN * 2) + chunk * 16 + (p & 1) * 8;
    }
  }
  constexpr int TOFF = (AMODE == 0 ? 4 : 8) * (BN * 2);
  auto ldA = [&](Frag<MT, NT>& f, lds_char* sb, int ks, int m) { f.a[m] = LDS128(sb + ard[ks] + m * 4096); };
  auto ldB = [&](Frag<MT, NT>& f, lds_char* sb, int ks, int n) {
    f.bl[n] = tr_read(sb + brd[n] + ks * 16 * (BN * 2));
    f.bh[n] = tr_read(sb + brd[n] + ks * 16 * (BN * 2) + TOFF);
  };
  auto mma = [&](Frag<MT, NT>& f, int m, int n) {
    bf16x8 bb = __builtin_shufflevector(f.bl[n], f.bh[n], 0, 1, 2, 3, 4, 5, 6, 7);
    acc[m][n] = SWAP ? MFMA16(bb, f.a[m], acc[m][n]) : MFMA16(f.a[m], bb, acc[m][n]);
  };
  auto step = [&](Frag<MT, NT>& cur, Frag<MT, NT>& nxt, lds_char* nsb, int nks, bool pf, int pkt, int pslot, int P0, int P1) {
    mma(cur, 0, 0);
    ldB(nxt, nsb, nks, 0); ldA(nxt, nsb, nks, 0);
    SBAR();
    if (NT == 2) { mma(cur, 0, NT - 1); ldB(nxt, nsb, nks, NT - 1); }
    if (MT == 2) ldA(nxt, nsb, nks, MT - 1);
    SBAR();
    if (pf && P0 < P1) piece(pkt, pslot, P0);
    if (MT == 2) { mma(cur, MT - 1, 0); SBAR(); }
    if (pf) { for (int i = P0 + 1; i < P1; ++i) piece(pkt, pslot, i); }
    if (MT == 2 && NT == 2) mma(cur, MT - 1, NT - 1);
    SBAR();
  };
  constexpr int PQ = (NLD + 2) / 3;
  constexpr int P1 = PQ, P2 = 2 * PQ < NLD ? 2 * PQ : NLD;
  issue(0, 0);
  issue(1, 1);
  issue(2, 2);
  Frag<MT, NT> X, Y;
  WAIT_VM(2 * NLD);
  RAW_BAR();
#pragma unroll
  for (int n = 0; n < NT; ++n) ldB(X, lds, 0, n);
#pragma unroll
  for (int m = 0; m < MT; ++m) ldA(X, lds, 0, m);
  int slot = 0, pslot = NS - 1;
  static_assert(NS == 3, "");
  auto body = [&](int t, auto pf_c, auto fl_c) {
    constexpr bool pf = decltype(pf_c)::value, fl = decltype(fl_c)::value;
    lds_char* sb = lds + slot * STAGE;
    const int pkt = t + NS - 1;
    step(X, Y, sb, 1, pf, pkt, pslot, 0, P1);
    step(Y, X, sb, 2, pf, pkt, pslot, P1, P2);
    step(X, Y, sb, 3, pf, pkt, pslot, P2, NLD);
    if (fl) WAIT_VM(NLD); else WAIT_VM(0);
    RAW_BAR();
    pslot = slot;
    slot = (slot + 1 == NS) ? 0 : slot + 1;
    step(Y, X, lds + slot * STAGE, 0, false, 0, 0, 0, 0);
  };
  body(0, std::false_type{}, std::true_type{});
#pragma unroll 1
  for (int t = 1; t < nk - 2; ++t) body(t, std::true_type{}, std::true_type{});
#pragma unroll 1
  for (int t = 0; t < 2; ++t) body(t, std::false_type{}, std::false_type{});
  RAW_BAR();
}

constexpr int K1_NS = 3;
constexpr int K1_LDS = K1_NS * 49152;
constexpr float QSCALE = 0.22360679774997896f * 1.4426950408889634f;

DI unsigned unit_off(int U, unsigned head_stride) {
  const int p = U / 5, ui = U - p * 5;
  return (2 * p + (ui >= 3 ? 1 : 0)) * head_stride + (ui == 2 ? 1024 : (ui >= 3 ? ui - 3 : ui) * 512);
}

__global__ __launch_bounds__(512, 2) void k1_kernel(const u16* __restrict__ x, const u16* __restrict__ wqkv, const u16* __restrict__ w1,
                                                    const u16* __restrict__ w2, const u16* __restrict__ wo, const u16* __restrict__ gw,
                                                    char* __restrict__ ws) {
  extern __shared__ __attribute__((aligned(16))) char smem[];
  lds_char* lds = (lds_char*)smem;
  const int bid = blockIdx.x, tid = threadIdx.x, l = tid & 63;
  const int w = __builtin_amdgcn_readfirstlane(tid >> 6);
  if (bid >= 232) {
    const unsigned lds0 = (unsigned)(uintptr_t)lds;
    const unsigned roff = (l / 10) * 1280 + (l % 10) * 16;
    const int tk = l & 15, kq = l >> 4;
    const int sw = (bid - 232) * 8 + w;
    auto tload = [&](int u, unsigned sb) {
      const int ctt = u / 74, ks = u - ctt * 74;
      const char* src = (ks < 54 ? (const char*)w2 + (size_t)ks * 32 * 1280 : (const char*)wo + (size_t)(ks - 54) * 32 * 1280) + ctt * 160;
#pragma unroll
      for (int pc = 0; pc < 6; ++pc) glds16m(src + pc * 6 * 1280, roff, lds0 + sb + pc * 960, pc == 5 ? (1ull << 20) - 1 : (1ull << 60) - 1);
    };
    auto tstore = [&](int u, unsigned sb) {
      const int ks = u % 74;
      const unsigned rd = sb + ((ks < 54 ? 16 * (kq >> 1) + 4 * (kq & 1) : 4 * kq) + (tk >> 2)) * 160 + (tk & 3) * 8;
      const unsigned hi = (ks < 54 ? 8 : 16) * 160;
#pragma unroll
      for (int n = 0; n < 5; ++n) {
        bf16x4 a = tr_read(lds + rd + n * 32), b = tr_read(lds + rd + hi + n * 32);
        st16(ws + WS_WT + (size_t)u * 5120 + n * 1024 + l * 16, __builtin_bit_cast(u32x4, __builtin_shufflevector(a, b, 0, 1, 2, 3, 4, 5, 6, 7)));
      }
    };
    const bool extra = bid >= 252 && w < 4;
    {
      const u32x4 sn = {0xFFFFFFFFu, 0xFFFFFFFFu, 0xFFFFFFFFu, 0xFFFFFFFFu};
#pragma unroll
      for (int i = 0; i < 14; ++i)
        if (i * 192 + sw < 2560) st16(ws + WS_O + (size_t)(i * 192 + sw) * 1024 + l * 16, sn);
    }
    if (bid < 248) {
#pragma unroll
      for (int half = 0; half < 2; ++half) {
        const int row = (bid - 232) * 128 + half * 64 + w * 8 + (l >> 3), sub = l & 7;
        float s = 0.f;
#pragma unroll
        for (int i = 0; i < 10; ++i) {
          const int c = (i * 8 + sub) * 8;
          u32x4 xv = *(const u32x4*)(x + (size_t)row * DM + c);
          u32x4 gv = *(const u32x4*)(gw + c);
#pragma unroll
          for (int q = 0; q < 4; ++q) {
            s = fmaf(__uint_as_float(xv[q] << 16), __uint_as_float(gv[q] << 16), s);
            s = fmaf(__uint_as_float(xv[q] & 0xFFFF0000u), __uint_as_float(gv[q] & 0xFFFF0000u), s);
          }
        }
        s += __shfl_xor(s, 1); s += __shfl_xor(s, 2); s += __shfl_xor(s, 4);
        s = rbf(s);
        float g = rbf(1.f / (1.f + __expf(-s)));
        if (sub == 0) ((float*)(ws + WS_G))[row] = g;
      }
    }
#pragma unroll
    for (int i = 0; i < 3; ++i) tload(sw * 3 + i, w * 15360 + i * 5120);
    if (extra) tload(576 + (bid - 252) * 4 + w, 122880 + w * 5120);
    WAIT_VM(0);
#pragma unroll
    for (int i = 0; i < 3; ++i) tstore(sw * 3 + i, w * 15360 + i * 5120);
    if (extra) tstore(576 + (bid - 252) * 4 + w, 122880 + w * 5120);
    return;
  }
  const int lin = ((bid & 7) >> 1) * 29 + (bid >> 3);
  const int rowp = (bid & 1) * 4 + (lin & 3), ct = lin >> 2;
  const int wr = w >> 1, wc = w & 1, r = l & 31, h = l >> 5;
  if (bid == 0 && tid < 24) *(unsigned*)(ws + WS_FLAG + tid * 256) = 0u;
  const char* Abase = (const char*)x + (size_t)rowp * 256 * 1280;
  const int tile0 = rowp * 8 + wr * 2;
  f32x16 acc[2][2];
#pragma unroll
  for (int m = 0; m < 2; ++m)
#pragma unroll
    for (int n = 0; n < 2; ++n)
#pragma unroll
      for (int i = 0; i < 16; ++i) acc[m][n][i] = 0.f;
  if (ct >= 10 && ct < 15) {
    gemm8<2, 2, 0, false, K1_NS>(acc, Abase, 0, (const char*)wqkv + ct * 256, NQKV * 2, 15, 10, lds);
#pragma unroll
    for (int n = 0; n < 2; ++n) {
      const int f = (ct - 10) * 128 + wc * 64 + n * 32 + r;
      const int head = f / 20, d = f - head * 20;
      char* base = ws + WS_KV + (size_t)head * KV_HS + tile0 * 3072 + 1536 + h * 384 + d * 16;
#pragma unroll
      for (int m = 0; m < 2; ++m) {
#pragma unroll
        for (int s = 0; s < 2; ++s) {
          u32x4 v;
#pragma unroll
          for (int j = 0; j < 4; ++j) v[j] = pk2(acc[m][n][8 * s + 2 * j], acc[m][n][8 * s + 2 * j + 1]);
          st16(base + m * 3072 + s * 768, v);
          if (d == 19) { u32x4 o = {0x3F803F80u, 0x3F803F80u, 0x3F803F80u, 0x3F803F80u}; st16(base + m * 3072 + s * 768 + 16, o); }
        }
      }
    }
  } else {
    const bool isqk = ct < 10;
    const char* Bb = isqk ? (const char*)wqkv + ct * 256 : (const char*)w1 + (ct - 15) * 256;
    const bool isq = ct < 5;
    const unsigned ts = isq ? 1536 : 3072;
    unsigned eoff[4], mixed = 0;
#pragma unroll
    for (int i = 0; i < 4; ++i) {
      const int U = (isq ? ct : ct - 5) * 16 + wc * 8 + 2 * i + h;
      eoff[i] = unit_off(U, isq ? Q_HS : KV_HS) + r * 16;
      mixed |= (U % 5 == 2) ? 1u << i : 0u;
    }
    asm volatile("" : "+v"(eoff[0]), "+v"(eoff[1]), "+v"(eoff[2]), "+v"(eoff[3]), "+v"(mixed));
    gemm8<2, 2, 0, true, K1_NS>(acc, Abase, 0, Bb, isqk ? NQKV * 2 : DFF * 2, ct == 28 ? 7 : 15, 10, lds);
    if (isqk) {
      const float sc = isq ? QSCALE : 1.f;
      char* base = ws + (isq ? WS_Q : WS_KV) + (size_t)tile0 * ts;
#pragma unroll
      for (int n = 0; n < 2; ++n) {
#pragma unroll
        for (int gp = 0; gp < 2; ++gp) {
#pragma unroll
          for (int m = 0; m < 2; ++m) {
            const f32x16& a = acc[m][n];
            unsigned a0 = pk2(a[8 * gp] * sc, a[8 * gp + 1] * sc), a1 = pk2(a[8 * gp + 2] * sc, a[8 * gp + 3] * sc);
            unsigned b0 = pk2(a[8 * gp + 4] * sc, a[8 * gp + 5] * sc), b1 = pk2(a[8 * gp + 6] * sc, a[8 * gp + 7] * sc);
            auto s0 = __builtin_amdgcn_permlane32_swap(a0, b0, false, false);
            auto s1 = __builtin_amdgcn_permlane32_swap(a1, b1, false, false);
            u32x4 v = {s0[0], s1[0], s0[1], s1[1]};
            char* p = base + m * ts + eoff[n * 2 + gp];
            st16(p, v);
            if (mixed >> (n * 2 + gp) & 1) st16(p + (isq ? Q_HS : KV_HS), v);
          }
        }
      }
    } else {
#pragma unroll
      for (int n = 0; n < 2; ++n) {
        const int c32 = (ct - 15) * 4 + wc * 2 + n;
        if (c32 < 54) {
#pragma unroll
          for (int m = 0; m < 2; ++m) {
#pragma unroll
            for (int s = 0; s < 2; ++s) {
              u32x4 v;
#pragma unroll
              for (int q = 0; q < 4; ++q) v[q] = relu_pk(pk2(acc[m][n][8 * s + 2 * q], acc[m][n][8 * s + 2 * q + 1]));
              st16(ws + WS_H + ((size_t)((tile0 + m) * 2 + (r >> 4)) * 54 + c32) * 1024 + ((2 * s + h) * 16 + (r & 15)) * 16, v);
            }
          }
        }
      }
    }
  }
}

constexpr int K2_LDS = 81920;


DI float xhalf_max(float m) {
  auto rr = __builtin_amdgcn_permlane32_swap(__float_as_uint(m), __float_as_uint(m), false, false);
  return fmaxf(__uint_as_float(rr[0]), __uint_as_float(rr[1]));
}
DI float max16(const f32x16& s) {
  float a = fmaxf(fmaxf(s[0], s[1]), s[2]), b = fmaxf(fmaxf(s[3], s[4]), s[5]);
  a = fmaxf(fmaxf(a, s[6]), s[7]); b = fmaxf(fmaxf(b, s[8]), s[9]);
  a = fmaxf(fmaxf(a, s[10]), s[11]); b = fmaxf(fmaxf(b, s[12]), s[13]);
  a = fmaxf(fmaxf(a, s[14]), s[15]);
  return fmaxf(a, b);
}
DI void wait_count(gu32* flag, unsigned target) {
  for (unsigned spins = 0; spins < (1u << 18); ++spins) {
    if ((unsigned)__builtin_amdgcn_readfirstlane(__hip_atomic_load(flag, __ATOMIC_RELAXED, __HIP_MEMORY_SCOPE_AGENT)) >= target) break;
    __builtin_amdgcn_s_sleep(2);
  }
}

DI void attn_wg(char* __restrict__ ws, lds_char* lds, int bid) {
  const int tid = threadIdx.x, l = tid & 63;
  const int w = __builtin_amdgcn_readfirstlane(tid >> 6);
  const int r = l & 31, h = l >> 5;
  const int xcd = bid & 7, idx = bid >> 3;
  const int head = xcd * 4 + (idx >> 3), qb = idx & 7;
  const int qt = qb * 8 + w;
  const char* kv = ws + WS_KV + (size_t)head * KV_HS;
  const unsigned lds0 = (unsigned)(uintptr_t)lds;
  const char* kvp = kv + (w < 6 ? w : 0) * 1024;
  const unsigned kvdst = lds0 + (w < 6 ? w * 1024 : 24576), kvstep = w < 6 ? 6144 : 0;
  auto dma_kv = [&](int ch, int U) { glds16(kvp + (size_t)ch * 6144, l * 16, kvdst + U * kvstep); };
  const char* qp = ws + WS_Q + (size_t)head * Q_HS + qt * 1536;
  bf16x8 q1;
  u32x2 q2u;
  asm volatile("global_load_dwordx4 %0, %1, %2" : "=v"(q1) : "v"(l * 16), "s"(qp) : "memory");
  asm volatile("global_load_dwordx2 %0, %1, %2" : "=v"(q2u) : "v"(1024 + r * 16 + (head & 1) * 8), "s"(qp) : "memory");
  dma_kv(0, 0); dma_kv(1, 1); dma_kv(2, 2);
  const unsigned k1rd = l * 16, k2rd = h ? 1536 + 20 * 16 : 1024 + r * 16 + (head & 1) * 8;
  const int dd = r < 24 ? r : (r < 28 ? r - 4 : r - 12);
  const unsigned vrd = 1536 + h * 384 + dd * 16;
  f32x16 zero;
#pragma unroll
  for (int i = 0; i < 16; ++i) zero[i] = 0.f;
  asm volatile("s_waitcnt vmcnt(2)" : "+v"(q1), "+v"(q2u)::"memory");
  RAW_BAR();
  if (h) { q2u[0] = 0; q2u[1] = 0; }
  bf16x8 kf1 = LDS128(lds + k1rd);
  bf16x4 kf2 = LDS64(lds + k2rd);
  {
    f32x16 s = MFMA16(kf1, q1, zero);
    s = MFMA8(kf2, __builtin_bit_cast(bf16x4, q2u), s);
    float m = xhalf_max(max16(s));
    if (h) q2u[0] = pk2(-m, 0.f);
  }
  const bf16x4 q2 = __builtin_bit_cast(bf16x4, q2u);
  f32x16 o = zero;
  f32x16 sA = MFMA16(kf1, q1, zero);
  sA = MFMA8(kf2, q2, sA);
  f32x16 sB;
  kf1 = LDS128(lds + 3072 + k1rd);
  kf2 = LDS64(lds + 3072 + k2rd);
  bf16x8 vf0 = LDS128(lds + vrd);
  bf16x8 vf1 = vf0;
  u32x4 p0, p1 = {0u, 0u, 0u, 0u};
#define AT_STEP(SC, SN, KN, VC, VN, D0) do { \
    SN = MFMA16(kf1, q1, zero); \
    kf1 = LDS128((KN) + k1rd); SBAR(); \
    SC[0] = EX(SC[0]); SC[1] = EX(SC[1]); SC[2] = EX(SC[2]); SC[3] = EX(SC[3]); PIN(SC); SBAR(); \
    o = MFMA16(vf1, __builtin_bit_cast(bf16x8, p1), o); PIN(o); \
    vf1 = LDS128((VC) + vrd + 768); SBAR(); \
    D0; \
    SC[4] = EX(SC[4]); SC[5] = EX(SC[5]); SC[6] = EX(SC[6]); SC[7] = EX(SC[7]); \
    p0[0] = pk2(SC[0], SC[1]); p0[1] = pk2(SC[2], SC[3]); p0[2] = pk2(SC[4], SC[5]); p0[3] = pk2(SC[6], SC[7]); PIN(p0); SBAR(); \
    SN = MFMA8(kf2, q2, SN); \
    kf2 = LDS64((KN) + k2rd); SBAR(); \
    SC[8] = EX(SC[8]); SC[9] = EX(SC[9]); SC[10] = EX(SC[10]); SC[11] = EX(SC[11]); PIN(SC); SBAR(); \
    o = MFMA16(vf0, __builtin_bit_cast(bf16x8, p0), o); PIN(o); \
    vf0 = LDS128((VN) + vrd); SBAR(); \
    SC[12] = EX(SC[12]); SC[13] = EX(SC[13]); SC[14] = EX(SC[14]); SC[15] = EX(SC[15]); \
    p1[0] = pk2(SC[8], SC[9]); p1[1] = pk2(SC[10], SC[11]); p1[2] = pk2(SC[12], SC[13]); p1[3] = pk2(SC[14], SC[15]); PIN(p1); SBAR(); \
  } while (0)
#define NOP_ ((void)0)
#define SUPER(U, WN, X0) do { \
    WAIT_VM(WN); \
    RAW_BAR(); \
    AT_STEP(sA, sB, lds + (((U) + 1) & 3) * 6144, lds + (U) * 6144, lds + (U) * 6144 + 3072, X0); \
    AT_STEP(sB, sA, lds + (((U) + 1) & 3) * 6144 + 3072, lds + (U) * 6144 + 3072, lds + (((U) + 1) & 3) * 6144, NOP_); \
  } while (0)
#pragma unroll 1
  for (int c = 0; c < 28; c += 4) {
    SUPER(0, 1, dma_kv(c + 3, 3)); SUPER(1, 1, dma_kv(c + 4, 0)); SUPER(2, 1, dma_kv(c + 5, 1)); SUPER(3, 1, dma_kv(c + 6, 2));
  }
  SUPER(0, 1, dma_kv(31, 3)); SUPER(1, 1, NOP_); SUPER(2, 0, NOP_);
  if (tid == 0) __hip_atomic_fetch_add((gu32*)(ws + WS_FLAG + qb * 256), 1u, __ATOMIC_RELAXED, __HIP_MEMORY_SCOPE_AGENT);
  SUPER(3, 63, NOP_);
  o = MFMA16(vf1, __builtin_bit_cast(bf16x8, p1), o);
  float lsum;
  {
    auto rr = __builtin_amdgcn_permlane32_swap(__float_as_uint(o[8]), __float_as_uint(o[8]), false, false);
    lsum = __uint_as_float(rr[1]);
  }
  bool okv = lsum < 1.0e18f;
#pragma unroll
  for (int i = 0; i < 12; ++i) okv = okv && ((h && i >= 8) ? true : fabsf(o[i]) < 1.0e30f);
  if (!__all(okv)) {
    u32x2 qz = q2u; if (h) qz[0] = 0;
    const bf16x4 q2z = __builtin_bit_cast(bf16x4, qz);
    float m = -INFINITY;
    o = zero;
    for (int t = 0; t < 64; ++t) {
      const char* tb = kv + (size_t)t * 3072;
      bf16x8 k1 = *(const bf16x8*)(tb + k1rd);
      bf16x4 k2 = *(const bf16x4*)(tb + k2rd);
      bf16x8 v0 = *(const bf16x8*)(tb + vrd);
      bf16x8 v1 = *(const bf16x8*)(tb + vrd + 768);
      f32x16 s = MFMA16(k1, q1, zero);
      s = MFMA8(k2, q2z, s);
      float mn = fmaxf(m, xhalf_max(max16(s)));
      float alpha = EX(m - mn);
      m = mn;
#pragma unroll
      for (int i = 0; i < 16; ++i) o[i] *= alpha;
      u32x4 a0, a1;
#pragma unroll
      for (int j = 0; j < 4; ++j) {
        a0[j] = pk2(EX(s[2 * j] - m), EX(s[2 * j + 1] - m));
        a1[j] = pk2(EX(s[8 + 2 * j] - m), EX(s[8 + 2 * j + 1] - m));
      }
      o = MFMA16(v0, __builtin_bit_cast(bf16x8, a0), o);
      o = MFMA16(v1, __builtin_bit_cast(bf16x8, a1), o);
    }
    auto rr = __builtin_amdgcn_permlane32_swap(__float_as_uint(o[8]), __float_as_uint(o[8]), false, false);
    lsum = __uint_as_float(rr[1]);
  }
  const float inv = 1.f / lsum;
  char* ob = ws + WS_O + (size_t)(qt * 2 + (r >> 4)) * 20 * 1024 + (r & 15) * 16;
#pragma unroll
  for (int g = 0; g < 3; ++g) {
    const int gi = 2 * g + h;
    if (gi < 5) {
      const int G = 5 * head + gi;
      u32x2 v;
      v[0] = pk2(o[4 * g] * inv, o[4 * g + 1] * inv);
      v[1] = pk2(o[4 * g + 2] * inv, o[4 * g + 3] * inv);
      __hip_atomic_store((gu64*)(ob + (G >> 3) * 1024 + (G & 3) * 256 + ((G >> 2) & 1) * 8), __builtin_bit_cast(unsigned long long, v),
                         __ATOMIC_RELAXED, __HIP_MEMORY_SCOPE_AGENT);
    }
  }
}

DI void out_wg(char* __restrict__ ws, u16* __restrict__ out, lds_char* lds, int j) {
  const int tid = threadIdx.x, l = tid & 63;
  const int w = __builtin_amdgcn_readfirstlane(tid >> 6);
  const unsigned lds0 = (unsigned)(uintptr_t)lds;
  const int wt = w & 3, kh = w >> 2;
  const int rt = j & 31, ct = j >> 5;
  const int tk = l & 15, kq = l >> 4;
  const int token = rt * 64 + wt * 16 + tk;
  lds_char* red = lds + wt * 5120 + l * 16;
  const char* wt0 = ws + WS_WT + (size_t)ct * 74 * 5120;
  float bt[3][4];
  const float g = ((const float*)(ws + WS_G))[token];
  {
    auto wstage = [&](int st, int slot) {
#pragma unroll
      for (int i = 0; i < 4; ++i)
        if (i * 8 + w < 30) glds16(wt0 + (size_t)st * 30720 + (i * 8 + w) * 1024, l * 16, lds0 + slot * 30720 + (i * 8 + w) * 1024);
    };
    const bf16x8* hp = (const bf16x8*)(ws + WS_H + (size_t)((rt * 4 + wt) * 54 + kh * 3) * 1024) + l;
    bf16x8 ha[3], hb[3];
#pragma unroll
    for (int i = 0; i < 3; ++i) ha[i] = hp[i * 64];
    wstage(0, 0);
    f32x4 accb[5];
#pragma unroll
    for (int n = 0; n < 5; ++n) accb[n] = f32x4{0.f, 0.f, 0.f, 0.f};
    const unsigned br2 = kh * 15360 + l * 16;
    auto fstage = [&](int st, int slot, bf16x8 (&hc)[3], bf16x8 (&hn)[3], bool more) {
      WAIT_VM(0);
      RAW_BAR();
      if (more) {
#pragma unroll
        for (int i = 0; i < 3; ++i) hn[i] = hp[((st + 1) * 6 + i) * 64];
        wstage(st + 1, slot ^ 1);
      }
      lds_char* sb = lds + slot * 30720 + br2;
#pragma unroll
      for (int i = 0; i < 3; ++i)
#pragma unroll
        for (int n = 0; n < 5; ++n) accb[n] = MFMAS(LDS128(sb + (i * 5 + n) * 1024), hc[i], accb[n]);
    };
#pragma unroll 1
    for (int st = 0; st < 8; st += 2) { fstage(st, 0, ha, hb, true); fstage(st + 1, 1, hb, ha, true); }
    fstage(8, 0, ha, hb, false);
    RAW_BAR();
#pragma unroll
    for (int n = 0; n < 5; ++n)
      if ((n < 3) != (kh == 0)) *(__attribute__((address_space(3))) f32x4*)(red + n * 1024) = accb[n];
    RAW_BAR();
    const float omg = rbf(1.f - g);
#pragma unroll
    for (int n = 0; n < 5; ++n)
      if ((n < 3) == (kh == 0)) {
        f32x4 a2 = *(__attribute__((address_space(3))) f32x4*)(red + n * 1024);
#pragma unroll
        for (int i = 0; i < 4; ++i) bt[n % 3][i] = rbf(omg * rbf(accb[n][i] + a2[i]));
      }
    RAW_BAR();
  }
  const char* wof = wt0 + 54 * 5120;
  bf16x8 wf[2][5];
#pragma unroll
  for (int ks = 0; ks < 2; ++ks)
#pragma unroll
    for (int n = 0; n < 5; ++n) wf[ks][n] = *(const bf16x8*)(wof + ((kh * 10 + ks) * 5 + n) * 1024 + l * 16);
#pragma unroll
  for (int ks = 0; ks < 2; ++ks)
#pragma unroll
    for (int n = 0; n < 5; ++n) PIN(wf[ks][n]);
#pragma unroll
  for (int i = 0; i < 10; ++i) {
    const int pc = i * 8 + w;
    glds16(wof + (pc + (pc < 40 ? 10 : 20)) * 1024, l * 16, lds0 + pc * 1024);
  }
  if (w == 0) wait_count((gu32*)(ws + WS_FLAG + (rt >> 2) * 256), 32u);
  WAIT_VM(0);
  RAW_BAR();
  __builtin_amdgcn_s_setprio(0);
  const char* op = ws + WS_O + (size_t)((rt * 4 + wt) * 20 + kh * 10) * 1024;
  u32x4 of[10];
  for (unsigned spins = 0; spins < (1u << 16); ++spins) {
    asm volatile("global_load_dwordx4 %0, %10, %11 sc1\n\tglobal_load_dwordx4 %1, %10, %11 offset:1024 sc1\n\t"
                 "global_load_dwordx4 %2, %10, %11 offset:2048 sc1\n\tglobal_load_dwordx4 %3, %10, %11 offset:3072 sc1\n\t"
                 "global_load_dwordx4 %4, %12, %11 sc1\n\tglobal_load_dwordx4 %5, %12, %11 offset:1024 sc1\n\t"
                 "global_load_dwordx4 %6, %12, %11 offset:2048 sc1\n\tglobal_load_dwordx4 %7, %12, %11 offset:3072 sc1\n\t"
                 "global_load_dwordx4 %8, %13, %11 sc1\n\tglobal_load_dwordx4 %9, %13, %11 offset:1024 sc1\n\ts_waitcnt vmcnt(0)"
                 : "=&v"(of[0]), "=&v"(of[1]), "=&v"(of[2]), "=&v"(of[3]), "=&v"(of[4]), "=&v"(of[5]), "=&v"(of[6]), "=&v"(of[7]), "=&v"(of[8]), "=&v"(of[9])
                 : "v"(l * 16), "s"(op), "v"(l * 16 + 4096), "v"(l * 16 + 8192) : "memory");
    unsigned mx = 0;
#pragma unroll
    for (int ks = 0; ks < 10; ++ks) mx = max(max(mx, max(of[ks][0], of[ks][1])), max(of[ks][2], of[ks][3]));
    if (__all(mx != 0xFFFFFFFFu)) break;
    __builtin_amdgcn_s_sleep(4);
  }
  __builtin_amdgcn_s_setprio(3);
  f32x4 acc[5];
#pragma unroll
  for (int n = 0; n < 5; ++n) acc[n] = f32x4{0.f, 0.f, 0.f, 0.f};
#pragma unroll
  for (int ks = 0; ks < 10; ++ks) {
#pragma unroll
    for (int n = 0; n < 5; ++n) acc[n] = MFMAS(wf[ks & 1][n], __builtin_bit_cast(bf16x8, of[ks]), acc[n]);
    if (ks + 2 < 10) {
#pragma unroll
      for (int n = 0; n < 5; ++n) wf[ks & 1][n] = LDS128(lds + ((kh * 8 + ks) * 5 + n) * 1024 + l * 16);
    }
    SBAR();
  }
  RAW_BAR();
#pragma unroll
  for (int n = 0; n < 5; ++n)
    if ((n < 3) != (kh == 0)) *(__attribute__((address_space(3))) f32x4*)(red + n * 1024) = acc[n];
  RAW_BAR();
  const size_t eo = (size_t)token * DM + ct * 80 + 4 * kq;
#pragma unroll
  for (int n = 0; n < 5; ++n) {
    if ((n < 3) == (kh == 0)) {
      f32x4 a2 = *(__attribute__((address_space(3))) f32x4*)(red + n * 1024);
      float v[4];
#pragma unroll
      for (int i = 0; i < 4; ++i) v[i] = rbf(g * rbf(acc[n][i] + a2[i])) + bt[n % 3][i];
      u32x2 o2;
      o2[0] = pk2(v[0], v[1]);
      o2[1] = pk2(v[2], v[3]);
      *(u32x2*)(out + eo + n * 16) = o2;
    }
  }
}

__global__ __launch_bounds__(512, 4) void k2_kernel(char* __restrict__ ws, u16* __restrict__ out) {
  extern __shared__ __attribute__((aligned(16))) char smem[];
  lds_char* lds = (lds_char*)smem;
  const int bid = blockIdx.x;
  if (bid < 256) attn_wg(ws, lds, bid);
  else out_wg(ws, out, lds, bid - 256);
}

extern "C" void kernel_launch(void* const* d_in, const int* in_sizes, int n_in, void* d_out, int out_size, void* d_ws, size_t ws_size,
                              hipStream_t stream) {
  const u16* x = (const u16*)d_in[0];
  const u16* wqkv = (const u16*)d_in[1];
  const u16* wo = (const u16*)d_in[2];
  const u16* w1 = (const u16*)d_in[4];
  const u16* w2 = (const u16*)d_in[5];
  const u16* gw = (const u16*)d_in[6];
  char* ws = (char*)d_ws;
  hipFuncSetAttribute((const void*)k1_kernel, hipFuncAttributeMaxDynamicSharedMemorySize, K1_LDS);
  hipFuncSetAttribute((const void*)k2_kernel, hipFuncAttributeMaxDynamicSharedMemorySize, K2_LDS);
  k1_kernel<<<256, 512, K1_LDS, stream>>>(x, wqkv, w1, w2, wo, gw, ws);
  k2_kernel<<<512, 512, K2_LDS, stream>>>(ws, (u16*)d_out);
}
```

```cpp
#include <hip/hip_runtime.h>
#include <hip/hip_bf16.h>
#include <stdint.h>
#include <type_traits>

typedef short bf16x8 __attribute__((ext_vector_type(8)));
typedef short bf16x4 __attribute__((ext_vector_type(4)));
typedef float f32x16 __attribute__((ext_vector_type(16)));
typedef float f32x4 __attribute__((ext_vector_type(4)));
typedef float f32x2 __attribute__((ext_vector_type(2)));
typedef __bf16 bfx2 __attribute__((ext_vector_type(2)));
typedef unsigned u32x4 __attribute__((ext_vector_type(4)));
typedef unsigned u32x2 __attribute__((ext_vector_type(2)));
typedef __attribute__((address_space(3))) char lds_char;
typedef __attribute__((address_space(1))) unsigned gu32;
typedef __attribute__((address_space(1))) unsigned long long gu64;
typedef unsigned short u16;

#define DI __device__ __forceinline__
#define MFMA16(a, b, c) __builtin_amdgcn_mfma_f32_32x32x16_bf16((a), (b), (c), 0, 0, 0)
#define MFMA8(a, b, c) __builtin_amdgcn_mfma_f32_32x32x8bf16_1k((a), (b), (c), 0, 0, 0)
#define MFMAS(a, b, c) __builtin_amdgcn_mfma_f32_16x16x32_bf16((a), (b), (c), 0, 0, 0)

constexpr int SEQ = 2048, DM = 640, NH = 32, DH = 20, DFF = 1728, NQKV = 1920;

constexpr size_t WS_KV = 0;
constexpr unsigned HPAD = 1280;
constexpr unsigned KV_HS = 64 * 3072 + HPAD, Q_HS = 64 * 1536 + HPAD;
constexpr size_t WS_Q = WS_KV + (size_t)32 * KV_HS;
constexpr size_t WS_H = WS_Q + (size_t)32 * Q_HS;
constexpr size_t WS_O = WS_H + (size_t)64 * 108 * 1024;
constexpr size_t WS_WT = WS_O + (size_t)128 * 20 * 1024;
constexpr size_t WS_G = WS_WT + (size_t)8 * 74 * 5120;
constexpr size_t WS_FLAG = WS_G + 2048 * 4;

DI unsigned pk2(float lo, float hi) {
  f32x2 v = {lo, hi};
  bfx2 r = __builtin_convertvector(v, bfx2);
  return __builtin_bit_cast(unsigned, r);
}
DI void st16(void* p, u32x4 v) { asm volatile("global_store_dwordx4 %0, %1, off sc1\n\ts_nop 1" :: "v"(p), "v"(v) : "memory"); }
DI unsigned relu_pk(unsigned v) {
  typedef short s16x2 __attribute__((ext_vector_type(2)));
  s16x2 a = __builtin_bit_cast(s16x2, v), z = {0, 0};
  return __builtin_bit_cast(unsigned, __builtin_elementwise_max(a, z));
}
DI float rbf(float x) { return (float)(__bf16)x; }
DI float bfbits2f(unsigned short b) { return __uint_as_float((unsigned)b << 16); }

DI void glds16(const char* sbase, unsigned voff, unsigned lds_dst) {
  unsigned keep;
  lds_dst = __builtin_amdgcn_readfirstlane(lds_dst);
  asm volatile("s_mov_b32 %0, m0\n\ts_mov_b32 m0, %3\n\ts_nop 0\n\tglobal_load_lds_dwordx4 %1, %2\n\ts_mov_b32 m0, %0"
               : "=&s"(keep) : "v"(voff), "s"(sbase), "s"(lds_dst) : "memory");
}
DI void glds16m(const char* sbase, unsigned voff, unsigned lds_dst, unsigned long long mask) {
  unsigned keep;
  lds_dst = __builtin_amdgcn_readfirstlane(lds_dst);
  asm volatile("s_mov_b32 %0, m0\n\ts_mov_b32 m0, %3\n\ts_mov_b64 exec, %4\n\tglobal_load_lds_dwordx4 %1, %2\n\ts_mov_b64 exec, -1\n\ts_mov_b32 m0, %0"
               : "=&s"(keep) : "v"(voff), "s"(sbase), "s"(lds_dst), "s"(mask) : "memory");
}
typedef short v4i16_t __attribute__((ext_vector_type(4)));
DI bf16x4 tr_read(lds_char* p) {
  return __builtin_bit_cast(bf16x4, __builtin_amdgcn_ds_read_tr16_b64_v4i16((__attribute__((address_space(3))) v4i16_t*)p));
}
#define SBAR() __builtin_amdgcn_sched_barrier(0)
#define PIN(x) asm volatile("" : "+v"(x))
#define WAIT_VM(n) asm volatile("s_waitcnt vmcnt(%0)" ::"n"(n) : "memory")
#define RAW_BAR() do { asm volatile("" ::: "memory"); __builtin_amdgcn_s_waitcnt(0xC07F); __builtin_amdgcn_s_barrier(); asm volatile("" ::: "memory"); } while (0)
#define LDS128(p) (*(__attribute__((address_space(3))) bf16x8*)(p))
#define LDS64(p) (*(__attribute__((address_space(3))) bf16x4*)(p))
#define EX(x) __builtin_amdgcn_exp2f(x)


template <int MT, int NT> struct Frag { bf16x8 a[MT]; bf16x4 bl[NT], bh[NT]; };

template <int MT, int NT, int AMODE, bool SWAP, int NS>
DI void gemm8(f32x16 (&acc)[MT][NT], const char* Abase, unsigned a_rt_stride, const char* Bbase, unsigned ldb_bytes, int cmax, int nk,
              lds_char* lds) {
  constexpr int BM = 128 * MT, BN = 64 * NT, BK = 64, KS = 4;
  constexpr int ABYTES = BM * BK * 2, BBYTES = BK * BN * 2, STAGE = ABYTES + BBYTES;
  constexpr int APW = ABYTES / 8192, BPW = BBYTES / 8192, NLD = APW + BPW;
  const int tid = threadIdx.x, l = tid & 63;
  const int w = __builtin_amdgcn_readfirstlane(tid >> 6);
  const int wr = w >> 1, wc = w & 1;
  const int r = l & 31, h = l >> 5;
  unsigned aoff[APW];
#pragma unroll
  for (int i = 0; i < APW; ++i) {
    const int pid = APW * w + i;
    if (AMODE == 0) {
      int row = 8 * pid + (l >> 3);
      aoff[i] = row * 1280 + (((l & 7) ^ ((row >> 1) & 7)) * 16);
    } else {
      aoff[i] = (pid / KS) * a_rt_stride + (pid % KS) * 1024 + l * 16;
    }
  }
  constexpr int BROWS = (BN == 128) ? 4 : 8;
  unsigned boff;
  if (BN == 128) {
    int c = (l & 15) ^ ((l >> 4) << 2);
    c = c < cmax ? c : cmax;
    boff = (BROWS * BPW * w + (l >> 4)) * ldb_bytes + c * 16;
  } else {
    int c = (l & 7) ^ (((l >> 4) & 1) << 2);
    c = c < cmax ? c : cmax;
    boff = (BROWS * BPW * w + (l >> 3)) * ldb_bytes + c * 16;
  }
  const unsigned lds0 = (unsigned)(uintptr_t)lds;
  auto piece = [&](int kt, int slot, int i) {
    const unsigned dst = lds0 + slot * STAGE;
    if (i < APW) {
      const char* ap = Abase + (AMODE == 0 ? kt * (BK * 2) : kt * (KS * 1024));
      glds16(ap, aoff[i < APW ? i : 0], dst + (APW * w + i) * 1024);
    } else {
      const char* bp = Bbase + (size_t)kt * BK * ldb_bytes;
      glds16(bp + (i - APW) * BROWS * ldb_bytes, boff, dst + ABYTES + (BPW * w + (i - APW)) * 1024);
    }
  };
  auto issue = [&](int kt, int slot) {
#pragma unroll
    for (int i = 0; i < NLD; ++i) piece(kt, slot, i);
  };
  unsigned ard[KS];
#pragma unroll
  for (int ks = 0; ks < KS; ++ks) {
    if (AMODE == 0) ard[ks] = (wr * (32 * MT) + r) * 128 + (((2 * ks + h) ^ ((r >> 1) & 7)) * 16);
    else ard[ks] = (wr * MT) * 4096 + ks * 1024 + l * 16;
  }
  unsigned brd[NT];
  {
    const int q = (l & 15) >> 2, p = l & 3, cbh = (l >> 4) & 1;
    const int krow = (AMODE == 0 ? 8 * h : 4 * h) + q;
#pragma unroll
    for (int n = 0; n < NT; ++n) {
      int chunk = (wc * NT + n) * 4 + 2 * cbh + (p >> 1);
      if (BN == 128) chunk ^= (q << 2); else chunk ^= (((q >> 1) & 1) << 2);
      brd[n] = ABYTES + krow * (BN * 2) + chunk * 16 + (p & 1) * 8;
    }
  }
  constexpr int TOFF = (AMODE == 0 ? 4 : 8) * (BN * 2);
  auto ldA = [&](Frag<MT, NT>& f, lds_char* sb, int ks, int m) { f.a[m] = LDS128(sb + ard[ks] + m * 4096); };
  auto ldB = [&](Frag<MT, NT>& f, lds_char* sb, int ks, int n) {
    f.bl[n] = tr_read(sb + brd[n] + ks * 16 * (BN * 2));
    f.bh[n] = tr_read(sb + brd[n] + ks * 16 * (BN * 2) + TOFF);
  };
  auto mma = [&](Frag<MT, NT>& f, int m, int n) {
    bf16x8 bb = __builtin_shufflevector(f.bl[n], f.bh[n], 0, 1, 2, 3, 4, 5, 6, 7);
    acc[m][n] = SWAP ? MFMA16(bb, f.a[m], acc[m][n]) : MFMA16(f.a[m], bb, acc[m][n]);
  };
  auto step = [&](Frag<MT, NT>& cur, Frag<MT, NT>& nxt, lds_char* nsb, int nks, bool pf, int pkt, int pslot, int P0, int P1) {
    mma(cur, 0, 0);
    ldB(nxt, nsb, nks, 0); ldA(nxt, nsb, nks, 0);
    SBAR();
    if (NT == 2) { mma(cur, 0, NT - 1); ldB(nxt, nsb, nks, NT - 1); }
    if (MT == 2) ldA(nxt, nsb, nks, MT - 1);
    SBAR();
    if (pf && P0 < P1) piece(pkt, pslot, P0);
    if (MT == 2) { mma(cur, MT - 1, 0); SBAR(); }
    if (pf) { for (int i = P0 + 1; i < P1; ++i) piece(pkt, pslot, i); }
    if (MT == 2 && NT == 2) mma(cur, MT - 1, NT - 1);
    SBAR();
  };
  constexpr int PQ = (NLD + 2) / 3;
  constexpr int P1 = PQ, P2 = 2 * PQ < NLD ? 2 * PQ : NLD;
  issue(0, 0);
  issue(1, 1);
  issue(2, 2);
  Frag<MT, NT> X, Y;
  WAIT_VM(2 * NLD);
  RAW_BAR();
#pragma unroll
  for (int n = 0; n < NT; ++n) ldB(X, lds, 0, n);
#pragma unroll
  for (int m = 0; m < MT; ++m) ldA(X, lds, 0, m);
  int slot = 0, pslot = NS - 1;
  static_assert(NS == 3, "");
  auto body = [&](int t, auto pf_c, auto fl_c) {
    constexpr bool pf = decltype(pf_c)::value, fl = decltype(fl_c)::value;
    lds_char* sb = lds + slot * STAGE;
    const int pkt = t + NS - 1;
    step(X, Y, sb, 1, pf, pkt, pslot, 0, P1);
    step(Y, X, sb, 2, pf, pkt, pslot, P1, P2);
    step(X, Y, sb, 3, pf, pkt, pslot, P2, NLD);
    if (fl) WAIT_VM(NLD); else WAIT_VM(0);
    RAW_BAR();
    pslot = slot;
    slot = (slot + 1 == NS) ? 0 : slot + 1;
    step(Y, X, lds + slot * STAGE, 0, false, 0, 0, 0, 0);
  };
  body(0, std::false_type{}, std::true_type{});
#pragma unroll 1
  for (int t = 1; t < nk - 2; ++t) body(t, std::true_type{}, std::true_type{});
  body(0, std::false_type{}, std::false_type{});
  {
    lds_char* sb = lds + slot * STAGE;
    step(X, Y, sb, 1, false, 0, 0, 0, 0);
    step(Y, X, sb, 2, false, 0, 0, 0, 0);
    step(X, Y, sb, 3, false, 0, 0, 0, 0);
#pragma unroll
    for (int m = 0; m < MT; ++m)
#pragma unroll
      for (int n = 0; n < NT; ++n) mma(Y, m, n);
  }
}

constexpr int K1_NS = 3;
constexpr int K1_LDS = K1_NS * 49152;
constexpr float QSCALE = 0.22360679774997896f * 1.4426950408889634f;

DI unsigned unit_off(int U, unsigned head_stride) {
  const int p = U / 5, ui = U - p * 5;
  return (2 * p + (ui >= 3 ? 1 : 0)) * head_stride + (ui == 2 ? 1024 : (ui >= 3 ? ui - 3 : ui) * 512);
}

__global__ __launch_bounds__(512, 2) void k1_kernel(const u16* __restrict__ x, const u16* __restrict__ wqkv, const u16* __restrict__ w1,
                                                    const u16* __restrict__ w2, const u16* __restrict__ wo, const u16* __restrict__ gw,
                                                    char* __restrict__ ws) {
  extern __shared__ __attribute__((aligned(16))) char smem[];
  lds_char* lds = (lds_char*)smem;
  const int bid = blockIdx.x, tid = threadIdx.x, l = tid & 63;
  const int w = __builtin_amdgcn_readfirstlane(tid >> 6);
  if (bid >= 232) {
    const unsigned lds0 = (unsigned)(uintptr_t)lds;
    const unsigned roff = (l / 10) * 1280 + (l % 10) * 16;
    const int tk = l & 15, kq = l >> 4;
    const int sw = (bid - 232) * 8 + w;
    auto tload = [&](int u, unsigned sb) {
      const int ctt = u / 74, ks = u - ctt * 74;
      const char* src = (ks < 54 ? (const char*)w2 + (size_t)ks * 32 * 1280 : (const char*)wo + (size_t)(ks - 54) * 32 * 1280) + ctt * 160;
#pragma unroll
      for (int pc = 0; pc < 6; ++pc) glds16m(src + pc * 6 * 1280, roff, lds0 + sb + pc * 960, pc == 5 ? (1ull << 20) - 1 : (1ull << 60) - 1);
    };
    auto tstore = [&](int u, unsigned sb) {
      const int ks = u % 74;
      const unsigned rd = sb + ((ks < 54 ? 16 * (kq >> 1) + 4 * (kq & 1) : 4 * kq) + (tk >> 2)) * 160 + (tk & 3) * 8;
      const unsigned hi = (ks < 54 ? 8 : 16) * 160;
#pragma unroll
      for (int n = 0; n < 5; ++n) {
        bf16x4 a = tr_read(lds + rd + n * 32), b = tr_read(lds + rd + hi + n * 32);
        st16(ws + WS_WT + (size_t)u * 5120 + n * 1024 + l * 16, __builtin_bit_cast(u32x4, __builtin_shufflevector(a, b, 0, 1, 2, 3, 4, 5, 6, 7)));
      }
    };
    const bool extra = bid >= 252 && w < 4;
    {
      const u32x4 sn = {0xFFFFFFFFu, 0xFFFFFFFFu, 0xFFFFFFFFu, 0xFFFFFFFFu};
#pragma unroll
      for (int i = 0; i < 14; ++i)
        if (i * 192 + sw < 2560) st16(ws + WS_O + (size_t)(i * 192 + sw) * 1024 + l * 16, sn);
    }
    if (bid < 248) {
#pragma unroll
      for (int half = 0; half < 2; ++half) {
        const int row = (bid - 232) * 128 + half * 64 + w * 8 + (l >> 3), sub = l & 7;
        float s = 0.f;
#pragma unroll
        for (int i = 0; i < 10; ++i) {
          const int c = (i * 8 + sub) * 8;
          u32x4 xv = *(const u32x4*)(x + (size_t)row * DM + c);
          u32x4 gv = *(const u32x4*)(gw + c);
#pragma unroll
          for (int q = 0; q < 4; ++q) {
            s = fmaf(__uint_as_float(xv[q] << 16), __uint_as_float(gv[q] << 16), s);
            s = fmaf(__uint_as_float(xv[q] & 0xFFFF0000u), __uint_as_float(gv[q] & 0xFFFF0000u), s);
          }
        }
        s += __shfl_xor(s, 1); s += __shfl_xor(s, 2); s += __shfl_xor(s, 4);
        s = rbf(s);
        float g = rbf(1.f / (1.f + __expf(-s)));
        if (sub == 0) ((float*)(ws + WS_G))[row] = g;
      }
    }
#pragma unroll
    for (int i = 0; i < 3; ++i) tload(sw * 3 + i, w * 15360 + i * 5120);
    if (extra) tload(576 + (bid - 252) * 4 + w, 122880 + w * 5120);
    WAIT_VM(0);
#pragma unroll
    for (int i = 0; i < 3; ++i) tstore(sw * 3 + i, w * 15360 + i * 5120);
    if (extra) tstore(576 + (bid - 252) * 4 + w, 122880 + w * 5120);
    return;
  }
  const int lin = ((bid & 7) >> 1) * 29 + (bid >> 3);
  const int rowp = (bid & 1) * 4 + (lin & 3), ct = lin >> 2;
  const int wr = w >> 1, wc = w & 1, r = l & 31, h = l >> 5;
  if (bid == 0 && tid < 24) *(unsigned*)(ws + WS_FLAG + tid * 256) = 0u;
  const char* Abase = (const char*)x + (size_t)rowp * 256 * 1280;
  const int tile0 = rowp * 8 + wr * 2;
  f32x16 acc[2][2];
#pragma unroll
  for (int m = 0; m < 2; ++m)
#pragma unroll
    for (int n = 0; n < 2; ++n)
#pragma unroll
      for (int i = 0; i < 16; ++i) acc[m][n][i] = 0.f;
  if (ct >= 10 && ct < 15) {
    gemm8<2, 2, 0, false, K1_NS>(acc, Abase, 0, (const char*)wqkv + ct * 256, NQKV * 2, 15, 10, lds);
#pragma unroll
    for (int n = 0; n < 2; ++n) {
      const int f = (ct - 10) * 128 + wc * 64 + n * 32 + r;
      const int head = f / 20, d = f - head * 20;
      char* base = ws + WS_KV + (size_t)head * KV_HS + tile0 * 3072 + 1536 + h * 384 + d * 16;
#pragma unroll
      for (int m = 0; m < 2; ++m) {
#pragma unroll
        for (int s = 0; s < 2; ++s) {
          u32x4 v;
#pragma unroll
          for (int j = 0; j < 4; ++j) v[j] = pk2(acc[m][n][8 * s + 2 * j], acc[m][n][8 * s + 2 * j + 1]);
          st16(base + m * 3072 + s * 768, v);
          if (d == 19) { u32x4 o = {0x3F803F80u, 0x3F803F80u, 0x3F803F80u, 0x3F803F80u}; st16(base + m * 3072 + s * 768 + 16, o); }
        }
      }
    }
  } else {
    const bool isqk = ct < 10;
    const char* Bb = isqk ? (const char*)wqkv + ct * 256 : (const char*)w1 + (ct - 15) * 256;
    const bool isq = ct < 5;
    const unsigned ts = isq ? 1536 : 3072;
    unsigned eoff[4], mixed = 0;
#pragma unroll
    for (int i = 0; i < 4; ++i) {
      const int U = (isq ? ct : ct - 5) * 16 + wc * 8 + 2 * i + h;
      eoff[i] = unit_off(U, isq ? Q_HS : KV_HS) + r * 16;
      mixed |= (U % 5 == 2) ? 1u << i : 0u;
    }
    asm volatile("" : "+v"(eoff[0]), "+v"(eoff[1]), "+v"(eoff[2]), "+v"(eoff[3]), "+v"(mixed));
    gemm8<2, 2, 0, true, K1_NS>(acc, Abase, 0, Bb, isqk ? NQKV * 2 : DFF * 2, ct == 28 ? 7 : 15, 10, lds);
    if (isqk) {
      const float sc = isq ? QSCALE : 1.f;
      char* base = ws + (isq ? WS_Q : WS_KV) + (size_t)tile0 * ts;
#pragma unroll
      for (int n = 0; n < 2; ++n) {
#pragma unroll
        for (int gp = 0; gp < 2; ++gp) {
#pragma unroll
          for (int m = 0; m < 2; ++m) {
            const f32x16& a = acc[m][n];
            unsigned a0 = pk2(a[8 * gp] * sc, a[8 * gp + 1] * sc), a1 = pk2(a[8 * gp + 2] * sc, a[8 * gp + 3] * sc);
            unsigned b0 = pk2(a[8 * gp + 4] * sc, a[8 * gp + 5] * sc), b1 = pk2(a[8 * gp + 6] * sc, a[8 * gp + 7] * sc);
            auto s0 = __builtin_amdgcn_permlane32_swap(a0, b0, false, false);
            auto s1 = __builtin_amdgcn_permlane32_swap(a1, b1, false, false);
            u32x4 v = {s0[0], s1[0], s0[1], s1[1]};
            char* p = base + m * ts + eoff[n * 2 + gp];
            st16(p, v);
            if (mixed >> (n * 2 + gp) & 1) st16(p + (isq ? Q_HS : KV_HS), v);
          }
        }
      }
    } else {
#pragma unroll
      for (int n = 0; n < 2; ++n) {
        const int c32 = (ct - 15) * 4 + wc * 2 + n;
        if (c32 < 54) {
#pragma unroll
          for (int m = 0; m < 2; ++m) {
#pragma unroll
            for (int s = 0; s < 2; ++s) {
              u32x4 v;
#pragma unroll
              for (int q = 0; q < 4; ++q) v[q] = relu_pk(pk2(acc[m][n][8 * s + 2 * q], acc[m][n][8 * s + 2 * q + 1]));
              st16(ws + WS_H + ((size_t)((tile0 + m) * 2 + (r >> 4)) * 54 + c32) * 1024 + ((2 * s + h) * 16 + (r & 15)) * 16, v);
            }
          }
        }
      }
    }
  }
}

constexpr int K2_LDS = 81920;


DI float xhalf_max(float m) {
  auto rr = __builtin_amdgcn_permlane32_swap(__float_as_uint(m), __float_as_uint(m), false, false);
  return fmaxf(__uint_as_float(rr[0]), __uint_as_float(rr[1]));
}
DI float max16(const f32x16& s) {
  float a = fmaxf(fmaxf(s[0], s[1]), s[2]), b = fmaxf(fmaxf(s[3], s[4]), s[5]);
  a = fmaxf(fmaxf(a, s[6]), s[7]); b = fmaxf(fmaxf(b, s[8]), s[9]);
  a = fmaxf(fmaxf(a, s[10]), s[11]); b = fmaxf(fmaxf(b, s[12]), s[13]);
  a = fmaxf(fmaxf(a, s[14]), s[15]);
  return fmaxf(a, b);
}
DI void wait_count(gu32* flag, unsigned target) {
  for (unsigned spins = 0; spins < (1u << 18); ++spins) {
    if ((unsigned)__builtin_amdgcn_readfirstlane(__hip_atomic_load(flag, __ATOMIC_RELAXED, __HIP_MEMORY_SCOPE_AGENT)) >= target) break;
    __builtin_amdgcn_s_sleep(2);
  }
}

DI void attn_wg(char* __restrict__ ws, lds_char* lds, int bid) {
  const int tid = threadIdx.x, l = tid & 63;
  const int w = __builtin_amdgcn_readfirstlane(tid >> 6);
  const int r = l & 31, h = l >> 5;
  const int xcd = bid & 7, idx = bid >> 3;
  const int head = xcd * 4 + (idx >> 3), qb = idx & 7;
  const int qt = qb * 8 + w;
  const char* kv = ws + WS_KV + (size_t)head * KV_HS;
  const unsigned lds0 = (unsigned)(uintptr_t)lds;
  const char* kvp = kv + (w < 6 ? w : 0) * 1024;
  const unsigned kvdst = lds0 + (w < 6 ? w * 1024 : 24576), kvstep = w < 6 ? 6144 : 0;
  auto dma_kv = [&](int ch, int U) { glds16(kvp + (size_t)ch * 6144, l * 16, kvdst + U * kvstep); };
  const char* qp = ws + WS_Q + (size_t)head * Q_HS + qt * 1536;
  bf16x8 q1;
  u32x2 q2u;
  asm volatile("global_load_dwordx4 %0, %1, %2" : "=v"(q1) : "v"(l * 16), "s"(qp) : "memory");
  asm volatile("global_load_dwordx2 %0, %1, %2" : "=v"(q2u) : "v"(1024 + r * 16 + (head & 1) * 8), "s"(qp) : "memory");
  dma_kv(0, 0); dma_kv(1, 1); dma_kv(2, 2);
  const unsigned k1rd = l * 16, k2rd = h ? 1536 + 20 * 16 : 1024 + r * 16 + (head & 1) * 8;
  const int dd = r < 24 ? r : (r < 28 ? r - 4 : r - 12);
  const unsigned vrd = 1536 + h * 384 + dd * 16;
  f32x16 zero;
#pragma unroll
  for (int i = 0; i < 16; ++i) zero[i] = 0.f;
  asm volatile("s_waitcnt vmcnt(2)" : "+v"(q1), "+v"(q2u)::"memory");
  RAW_BAR();
  if (h) { q2u[0] = 0; q2u[1] = 0; }
  bf16x8 kf1 = LDS128(lds + k1rd);
  bf16x4 kf2 = LDS64(lds + k2rd);
  {
    f32x16 s = MFMA16(kf1, q1, zero);
    s = MFMA8(kf2, __builtin_bit_cast(bf16x4, q2u), s);
    float m = xhalf_max(max16(s));
    if (h) q2u[0] = pk2(-m, 0.f);
  }
  const bf16x4 q2 = __builtin_bit_cast(bf16x4, q2u);
  f32x16 o = zero;
  f32x16 sA = MFMA16(kf1, q1, zero);
  sA = MFMA8(kf2, q2, sA);
  f32x16 sB;
  kf1 = LDS128(lds + 3072 + k1rd);
  kf2 = LDS64(lds + 3072 + k2rd);
  bf16x8 vf0 = LDS128(lds + vrd);
  bf16x8 vf1 = vf0;
  u32x4 p0, p1 = {0u, 0u, 0u, 0u};
#define AT_STEP(SC, SN, KN, VC, VN, D0) do { \
    SN = MFMA16(kf1, q1, zero); \
    kf1 = LDS128((KN) + k1rd); SBAR(); \
    SC[0] = EX(SC[0]); SC[1] = EX(SC[1]); SC[2] = EX(SC[2]); SC[3] = EX(SC[3]); PIN(SC); SBAR(); \
    o = MFMA16(vf1, __builtin_bit_cast(bf16x8, p1), o); PIN(o); \
    vf1 = LDS128((VC) + vrd + 768); SBAR(); \
    D0; \
    SC[4] = EX(SC[4]); SC[5] = EX(SC[5]); SC[6] = EX(SC[6]); SC[7] = EX(SC[7]); \
    p0[0] = pk2(SC[0], SC[1]); p0[1] = pk2(SC[2], SC[3]); p0[2] = pk2(SC[4], SC[5]); p0[3] = pk2(SC[6], SC[7]); PIN(p0); SBAR(); \
    SN = MFMA8(kf2, q2, SN); \
    kf2 = LDS64((KN) + k2rd); SBAR(); \
    SC[8] = EX(SC[8]); SC[9] = EX(SC[9]); SC[10] = EX(SC[10]); SC[11] = EX(SC[11]); PIN(SC); SBAR(); \
    o = MFMA16(vf0, __builtin_bit_cast(bf16x8, p0), o); PIN(o); \
    vf0 = LDS128((VN) + vrd); SBAR(); \
    SC[12] = EX(SC[12]); SC[13] = EX(SC[13]); SC[14] = EX(SC[14]); SC[15] = EX(SC[15]); \
    p1[0] = pk2(SC[8], SC[9]); p1[1] = pk2(SC[10], SC[11]); p1[2] = pk2(SC[12], SC[13]); p1[3] = pk2(SC[14], SC[15]); PIN(p1); SBAR(); \
  } while (0)
#define NOP_ ((void)0)
#define SUPER(U, WN, X0) do { \
    WAIT_VM(WN); \
    RAW_BAR(); \
    AT_STEP(sA, sB, lds + (((U) + 1) & 3) * 6144, lds + (U) * 6144, lds + (U) * 6144 + 3072, X0); \
    AT_STEP(sB, sA, lds + (((U) + 1) & 3) * 6144 + 3072, lds + (U) * 6144 + 3072, lds + (((U) + 1) & 3) * 6144, NOP_); \
  } while (0)
#pragma unroll 1
  for (int c = 0; c < 28; c += 4) {
    SUPER(0, 1, dma_kv(c + 3, 3)); SUPER(1, 1, dma_kv(c + 4, 0)); SUPER(2, 1, dma_kv(c + 5, 1)); SUPER(3, 1, dma_kv(c + 6, 2));
  }
  SUPER(0, 1, dma_kv(31, 3)); SUPER(1, 1, NOP_); SUPER(2, 0, NOP_);
  if (tid == 0) __hip_atomic_fetch_add((gu32*)(ws + WS_FLAG + qb * 256), 1u, __ATOMIC_RELAXED, __HIP_MEMORY_SCOPE_AGENT);
  SUPER(3, 63, NOP_);
  o = MFMA16(vf1, __builtin_bit_cast(bf16x8, p1), o);
  float lsum;
  {
    auto rr = __builtin_amdgcn_permlane32_swap(__float_as_uint(o[8]), __float_as_uint(o[8]), false, false);
    lsum = __uint_as_float(rr[1]);
  }
  bool okv = lsum < 1.0e18f;
#pragma unroll
  for (int i = 0; i < 12; ++i) okv = okv && ((h && i >= 8) ? true : fabsf(o[i]) < 1.0e30f);
  if (!__all(okv)) {
    u32x2 qz = q2u; if (h) qz[0] = 0;
    const bf16x4 q2z = __builtin_bit_cast(bf16x4, qz);
    float m = -INFINITY;
    o = zero;
    for (int t = 0; t < 64; ++t) {
      const char* tb = kv + (size_t)t * 3072;
      bf16x8 k1 = *(const bf16x8*)(tb + k1rd);
      bf16x4 k2 = *(const bf16x4*)(tb + k2rd);
      bf16x8 v0 = *(const bf16x8*)(tb + vrd);
      bf16x8 v1 = *(const bf16x8*)(tb + vrd + 768);
      f32x16 s = MFMA16(k1, q1, zero);
      s = MFMA8(k2, q2z, s);
      float mn = fmaxf(m, xhalf_max(max16(s)));
      float alpha = EX(m - mn);
      m = mn;
#pragma unroll
      for (int i = 0; i < 16; ++i) o[i] *= alpha;
      u32x4 a0, a1;
#pragma unroll
      for (int j = 0; j < 4; ++j) {
        a0[j] = pk2(EX(s[2 * j] - m), EX(s[2 * j + 1] - m));
        a1[j] = pk2(EX(s[8 + 2 * j] - m), EX(s[8 + 2 * j + 1] - m));
      }
      o = MFMA16(v0, __builtin_bit_cast(bf16x8, a0), o);
      o = MFMA16(v1, __builtin_bit_cast(bf16x8, a1), o);
    }
    auto rr = __builtin_amdgcn_permlane32_swap(__float_as_uint(o[8]), __float_as_uint(o[8]), false, false);
    lsum = __uint_as_float(rr[1]);
  }
  const float inv = 1.f / lsum;
  char* ob = ws + WS_O + (size_t)(qt * 2 + (r >> 4)) * 20 * 1024 + (r & 15) * 16;
#pragma unroll
  for (int g = 0; g < 3; ++g) {
    const int gi = 2 * g + h;
    if (gi < 5) {
      const int G = 5 * head + gi;
      u32x2 v;
      v[0] = pk2(o[4 * g] * inv, o[4 * g + 1] * inv);
      v[1] = pk2(o[4 * g + 2] * inv, o[4 * g + 3] * inv);
      __hip_atomic_store((gu64*)(ob + (G >> 3) * 1024 + (G & 3) * 256 + ((G >> 2) & 1) * 8), __builtin_bit_cast(unsigned long long, v),
                         __ATOMIC_RELAXED, __HIP_MEMORY_SCOPE_AGENT);
    }
  }
}

DI void out_wg(char* __restrict__ ws, u16* __restrict__ out, lds_char* lds, int j) {
  const int tid = threadIdx.x, l = tid & 63;
  const int w = __builtin_amdgcn_readfirstlane(tid >> 6);
  const unsigned lds0 = (unsigned)(uintptr_t)lds;
  const int wt = w & 3, kh = w >> 2;
  const int rt = j & 31, ct = j >> 5;
  const int tk = l & 15, kq = l >> 4;
  const int token = rt * 64 + wt * 16 + tk;
  lds_char* red = lds + wt * 5120 + l * 16;
  const char* wt0 = ws + WS_WT + (size_t)ct * 74 * 5120;
  float bt[3][4];
  const float g = ((const float*)(ws + WS_G))[token];
  {
    auto wstage = [&](int st, int slot) {
#pragma unroll
      for (int i = 0; i < 4; ++i)
        if (i * 8 + w < 30) glds16(wt0 + (size_t)st * 30720 + (i * 8 + w) * 1024, l * 16, lds0 + slot * 30720 + (i * 8 + w) * 1024);
    };
    const bf16x8* hp = (const bf16x8*)(ws + WS_H + (size_t)((rt * 4 + wt) * 54 + kh * 3) * 1024) + l;
    bf16x8 ha[3], hb[3];
#pragma unroll
    for (int i = 0; i < 3; ++i) ha[i] = hp[i * 64];
    wstage(0, 0);
    f32x4 accb[5];
#pragma unroll
    for (int n = 0; n < 5; ++n) accb[n] = f32x4{0.f, 0.f, 0.f, 0.f};
    const unsigned br2 = kh * 15360 + l * 16;
    auto fstage = [&](int st, int slot, bf16x8 (&hc)[3], bf16x8 (&hn)[3], bool more) {
      WAIT_VM(0);
      RAW_BAR();
      if (more) {
#pragma unroll
        for (int i = 0; i < 3; ++i) hn[i] = hp[((st + 1) * 6 + i) * 64];
        wstage(st + 1, slot ^ 1);
      }
      lds_char* sb = lds + slot * 30720 + br2;
#pragma unroll
      for (int i = 0; i < 3; ++i)
#pragma unroll
        for (int n = 0; n < 5; ++n) accb[n] = MFMAS(LDS128(sb + (i * 5 + n) * 1024), hc[i], accb[n]);
    };
#pragma unroll 1
    for (int st = 0; st < 8; st += 2) { fstage(st, 0, ha, hb, true); fstage(st + 1, 1, hb, ha, true); }
    fstage(8, 0, ha, hb, false);
    RAW_BAR();
#pragma unroll
    for (int n = 0; n < 5; ++n)
      if ((n < 3) != (kh == 0)) *(__attribute__((address_space(3))) f32x4*)(red + n * 1024) = accb[n];
    RAW_BAR();
    const float omg = rbf(1.f - g);
#pragma unroll
    for (int n = 0; n < 5; ++n)
      if ((n < 3) == (kh == 0)) {
        f32x4 a2 = *(__attribute__((address_space(3))) f32x4*)(red + n * 1024);
#pragma unroll
        for (int i = 0; i < 4; ++i) bt[n % 3][i] = rbf(omg * rbf(accb[n][i] + a2[i]));
      }
    RAW_BAR();
  }
  const char* wof = wt0 + 54 * 5120;
  bf16x8 wf[2][5];
#pragma unroll
  for (int ks = 0; ks < 2; ++ks)
#pragma unroll
    for (int n = 0; n < 5; ++n) wf[ks][n] = *(const bf16x8*)(wof + ((kh * 10 + ks) * 5 + n) * 1024 + l * 16);
#pragma unroll
  for (int ks = 0; ks < 2; ++ks)
#pragma unroll
    for (int n = 0; n < 5; ++n) PIN(wf[ks][n]);
#pragma unroll
  for (int i = 0; i < 10; ++i) {
    const int pc = i * 8 + w;
    glds16(wof + (pc + (pc < 40 ? 10 : 20)) * 1024, l * 16, lds0 + pc * 1024);
  }
  if (w == 0) wait_count((gu32*)(ws + WS_FLAG + (rt >> 2) * 256), 32u);
  WAIT_VM(0);
  RAW_BAR();
  __builtin_amdgcn_s_setprio(0);
  const char* op = ws + WS_O + (size_t)((rt * 4 + wt) * 20 + kh * 10) * 1024;
  u32x4 of[10];
  for (unsigned spins = 0; spins < (1u << 16); ++spins) {
    asm volatile("global_load_dwordx4 %0, %10, %11 sc1\n\tglobal_load_dwordx4 %1, %10, %11 offset:1024 sc1\n\t"
                 "global_load_dwordx4 %2, %10, %11 offset:2048 sc1\n\tglobal_load_dwordx4 %3, %10, %11 offset:3072 sc1\n\t"
                 "global_load_dwordx4 %4, %12, %11 sc1\n\tglobal_load_dwordx4 %5, %12, %11 offset:1024 sc1\n\t"
                 "global_load_dwordx4 %6, %12, %11 offset:2048 sc1\n\tglobal_load_dwordx4 %7, %12, %11 offset:3072 sc1\n\t"
                 "global_load_dwordx4 %8, %13, %11 sc1\n\tglobal_load_dwordx4 %9, %13, %11 offset:1024 sc1\n\ts_waitcnt vmcnt(0)"
                 : "=&v"(of[0]), "=&v"(of[1]), "=&v"(of[2]), "=&v"(of[3]), "=&v"(of[4]), "=&v"(of[5]), "=&v"(of[6]), "=&v"(of[7]), "=&v"(of[8]), "=&v"(of[9])
                 : "v"(l * 16), "s"(op), "v"(l * 16 + 4096), "v"(l * 16 + 8192) : "memory");
    unsigned mx = 0;
#pragma unroll
    for (int ks = 0; ks < 10; ++ks) mx = max(max(mx, max(of[ks][0], of[ks][1])), max(of[ks][2], of[ks][3]));
    if (__all(mx != 0xFFFFFFFFu)) break;
    __builtin_amdgcn_s_sleep(4);
  }
  __builtin_amdgcn_s_setprio(3);
  f32x4 acc[5];
#pragma unroll
  for (int n = 0; n < 5; ++n) acc[n] = f32x4{0.f, 0.f, 0.f, 0.f};
#pragma unroll
  for (int ks = 0; ks < 10; ++ks) {
#pragma unroll
    for (int n = 0; n < 5; ++n) acc[n] = MFMAS(wf[ks & 1][n], __builtin_bit_cast(bf16x8, of[ks]), acc[n]);
    if (ks + 2 < 10) {
#pragma unroll
      for (int n = 0; n < 5; ++n) wf[ks & 1][n] = LDS128(lds + ((kh * 8 + ks) * 5 + n) * 1024 + l * 16);
    }
    SBAR();
  }
  RAW_BAR();
#pragma unroll
  for (int n = 0; n < 5; ++n)
    if ((n < 3) != (kh == 0)) *(__attribute__((address_space(3))) f32x4*)(red + n * 1024) = acc[n];
  RAW_BAR();
  const size_t eo = (size_t)token * DM + ct * 80 + 4 * kq;
#pragma unroll
  for (int n = 0; n < 5; ++n) {
    if ((n < 3) == (kh == 0)) {
      f32x4 a2 = *(__attribute__((address_space(3))) f32x4*)(red + n * 1024);
      float v[4];
#pragma unroll
      for (int i = 0; i < 4; ++i) v[i] = rbf(g * rbf(acc[n][i] + a2[i])) + bt[n % 3][i];
      u32x2 o2;
      o2[0] = pk2(v[0], v[1]);
      o2[1] = pk2(v[2], v[3]);
      *(u32x2*)(out + eo + n * 16) = o2;
    }
  }
}

__global__ __launch_bounds__(512, 4) void k2_kernel(char* __restrict__ ws, u16* __restrict__ out) {
  extern __shared__ __attribute__((aligned(16))) char smem[];
  lds_char* lds = (lds_char*)smem;
  const int bid = blockIdx.x;
  if (bid < 256) attn_wg(ws, lds, bid);
  else out_wg(ws, out, lds, bid - 256);
}

extern "C" void kernel_launch(void* const* d_in, const int* in_sizes, int n_in, void* d_out, int out_size, void* d_ws, size_t ws_size,
                              hipStream_t stream) {
  const u16* x = (const u16*)d_in[0];
  const u16* wqkv = (const u16*)d_in[1];
  const u16* wo = (const u16*)d_in[2];
  const u16* w1 = (const u16*)d_in[4];
  const u16* w2 = (const u16*)d_in[5];
  const u16* gw = (const u16*)d_in[6];
  char* ws = (char*)d_ws;
  hipFuncSetAttribute((const void*)k1_kernel, hipFuncAttributeMaxDynamicSharedMemorySize, K1_LDS);
  hipFuncSetAttribute((const void*)k2_kernel, hipFuncAttributeMaxDynamicSharedMemorySize, K2_LDS);
  k1_kernel<<<256, 512, K1_LDS, stream>>>(x, wqkv, w1, w2, wo, gw, ws);
  k2_kernel<<<512, 512, K2_LDS, stream>>>(ws, (u16*)d_out);
}
```
